```python
import torch

def _softmax(x, axis=-1):
    return torch.softmax(x, dim=axis)
def _gelu(x):
    return torch.nn.functional.gelu(x, approximate="tanh")
def _silu(x):
    return torch.nn.functional.silu(x)
def _sigmoid(x):
    return torch.sigmoid(x)
def _layernorm(x, axis=-1, eps=1e-5):
    m = x.mean(dim=axis, keepdim=True)
    v = x.var(dim=axis, keepdim=True, unbiased=False)
    return (x - m) / torch.sqrt(v + eps)
def _scatter_add(tgt, idx, src, axis=0):
    return tgt.clone().index_add_(axis, idx, src)
def _segment_sum(src, seg, n):
    out = torch.zeros((n,) + src.shape[1:], dtype=src.dtype)
    return out.index_add_(0, seg, src)
def _pad(x, pw):
    flat = tuple(v for pair in reversed(pw) for v in pair)
    return torch.nn.functional.pad(x, flat)

kernel_name = 'sequential_preLN__ssm_conv_s4__dense_activation_mish__prompt'

def setup_inputs():

    _w = (lambda a: (lambda t: t.to(torch.bfloat16) if t.is_floating_point() else t)(torch.as_tensor(a)))
    def _A_setup():
        B, L, D, N = 1, 2048, 512, 128
        A_re = -(lambda _x: torch.exp(torch.as_tensor(_x)))((lambda *s: torch.randn(*s, dtype=torch.bfloat16) * 6.4)(D, N))
        A_im = torch.pi * (lambda *s: torch.randn(*s, dtype=torch.bfloat16) * 0.05)(D, N)
        B_re = (lambda *s: torch.randn(*s, dtype=torch.bfloat16) * 0.1)(D, N)
        B_im = (lambda *s: torch.randn(*s, dtype=torch.bfloat16) * 0.1)(D, N)
        C_re = (lambda *s: torch.randn(*s, dtype=torch.bfloat16) * 0.1)(D, N)
        C_im = (lambda *s: torch.randn(*s, dtype=torch.bfloat16) * 0.1)(D, N)
        log_dt = (lambda *s: torch.randn(*s, dtype=torch.bfloat16) * 0.05)(D) * 0.5 - 4.0
        Dskip = (lambda *s: torch.randn(*s, dtype=torch.bfloat16) * 0.05)(D)
        return {
            "u": _w((lambda *s: torch.randn(*s, dtype=torch.bfloat16) * 0.05)(B, L, D)),
            "A_re": _w(A_re),
            "A_im": _w(A_im),
            "B_re": _w(B_re),
            "B_im": _w(B_im),
            "C_re": _w(C_re),
            "C_im": _w(C_im),
            "log_dt": _w(log_dt),
            "Dskip": _w(Dskip),
        }
    _A_in = _A_setup()

    def _B_setup():
        B, S, D, F = 1, 2048, 512, 1792
        return {
            "x": _w((lambda *s: torch.randn(*s, dtype=torch.bfloat16) * 0.05)(B, S, D)),
            "w1": _w((lambda *s: torch.randn(*s, dtype=torch.bfloat16) * 0.8)(D, F) * (D ** -0.5)),
            "b1": _w((lambda *s: torch.randn(*s, dtype=torch.bfloat16) * 0.2)(F)),
            "w2": _w((lambda *s: torch.randn(*s, dtype=torch.bfloat16) * 0.2)(F, D) * (F ** -0.5)),
        }
    _B_in = _B_setup()

    return {
        "A_u": _A_in["u"],
        "A_A_re": _A_in["A_re"],
        "A_A_im": _A_in["A_im"],
        "A_B_re": _A_in["B_re"],
        "A_B_im": _A_in["B_im"],
        "A_C_re": _A_in["C_re"],
        "A_C_im": _A_in["C_im"],
        "A_log_dt": _A_in["log_dt"],
        "A_Dskip": _A_in["Dskip"],
        "B_x": _B_in["x"],
        "B_w1": _B_in["w1"],
        "B_b1": _B_in["b1"],
        "B_w2": _B_in["w2"],
    }

def reference(A_u, A_A_re, A_A_im, A_B_re, A_B_im, A_C_re, A_C_im, A_log_dt, A_Dskip, B_x, B_w1, B_b1, B_w2):
    def _A_ref(u, A_re, A_im, B_re, B_im, C_re, C_im, log_dt, Dskip):
        Bsz, L, D = u.shape
        dt = torch.exp(log_dt)[:, None]
        A = A_re + 1j * A_im
        Bc = B_re + 1j * B_im
        Cc = C_re + 1j * C_im
        logdA = dt * A
        dB = dt * Bc
        l = (lambda x, dt: x.to(dt))(torch.arange(L), A_re.dtype)
        P = torch.exp(logdA[:, :, None] * l[None, None, :])
        K = torch.einsum("dn,dnl,dn->dl", Cc, P, dB).real
        L2 = 2 * L
        Kp = _pad(K, ((0, 0), (0, L)))
        up = (lambda x, *d: x.permute(*d))(u, 0, 2, 1)
        up = _pad(up, ((0, 0), (0, 0), (0, L)))
        Kf = (lambda x, axes=(-2,-1): torch.fft.rfft2(x.float(), dim=axes))(Kp[:, None, :], axes=(-2, -1))
        Uf = (lambda x, axes=(-2,-1): torch.fft.rfft2(x.float(), dim=axes))(up[:, :, None, :], axes=(-2, -1))
        Yf = Uf * Kf[None, :, :, :]
        y = (lambda x, s, axes=(-2,-1): torch.fft.irfft2(x, s=s, dim=axes).to(torch.bfloat16))(Yf, (1, L2), axes=(-2, -1))[:, :, 0, :L]
        y = (lambda x, *d: x.permute(*d))(y, 0, 2, 1) + u * Dskip[None, None, :]
        return y

    def _B_ref(x, w1, b1, w2):
        h = torch.einsum("bsd,df->bsf", x, w1) + b1
        sp = torch.log1p(torch.exp(-torch.abs(h))) + torch.where(h > 0, h, torch.zeros_like(h))
        act = h * torch.tanh(sp)
        return torch.einsum("bsf,fd->bsd", act, w2)

    x = A_u
    h = x + _A_ref(_layernorm(x, axis=-1), A_A_re, A_A_im, A_B_re, A_B_im, A_C_re, A_C_im, A_log_dt, A_Dskip)
    h2 = h + _B_ref(_layernorm(h, axis=-1), B_w1, B_b1, B_w2)
    return h2
```

```cpp
#include <hip/hip_runtime.h>
#include <hip/hip_bf16.h>
#include <stdint.h>
typedef unsigned short u16;
static constexpr int L=2048, D=512, N=128, F=1792;
__device__ inline float bf2f(u16 v){ return __builtin_bit_cast(float, (uint32_t)v<<16); }
__device__ inline u16 f2bf(float f){ uint32_t u=__builtin_bit_cast(uint32_t,f); u += 0x7fff + ((u>>16)&1); return u>>16; }
__device__ inline float rbf(float f){ return bf2f(f2bf(f)); }

__global__ void k_ln(const u16* x, u16* u){
  int t=blockIdx.x, l=threadIdx.x; float v[8]; float s=0;
  for(int j=0;j<8;j++){ v[j]=bf2f(x[t*D+l*8+j]); s+=v[j]; }
  for(int o=32;o;o>>=1) s+=__shfl_xor(s,o);
  float m=s/D; float q=0;
  for(int j=0;j<8;j++){ q+=(v[j]-m)*(v[j]-m); }
  for(int o=32;o;o>>=1) q+=__shfl_xor(q,o);
  float var=rbf(q/D); float mb=rbf(m); float sd=rbf(sqrtf(rbf(var+1e-5f)));
  for(int j=0;j<8;j++) u[t*D+l*8+j]=f2bf(rbf(v[j]-mb)/sd);
}
__global__ void k_kgen(const u16* Are,const u16* Aim,const u16* Bre,const u16* Bim,const u16* Cre,const u16* Cim,const u16* logdt,float* K){
  int d=blockIdx.y; int l=blockIdx.x*256+threadIdx.x; float lf=rbf((float)l);
  float dt=rbf(expf(bf2f(logdt[d]))); float acc=0;
  for(int n=0;n<N;n++){ int i=d*N+n; float ar=dt*bf2f(Are[i]), ai=dt*bf2f(Aim[i]); float br=dt*bf2f(Bre[i]), bi=dt*bf2f(Bim[i]); float cr=bf2f(Cre[i]), ci=bf2f(Cim[i]);
    float e=expf(ar*lf); float sn,cs; sincosf(ai*lf,&sn,&cs); float pr=e*cs, pi=e*sn; float wr=cr*br-ci*bi, wi=cr*bi+ci*br; acc+=wr*pr-wi*pi; }
  K[d*L+l]=acc;
}
__global__ void k_conv(const u16* x,const u16* u,const float* K,const u16* Dskip,u16* h){
  int t=blockIdx.x; int d=blockIdx.y*256+threadIdx.x; float acc=0;
  for(int s=0;s<=t;s++) acc+=K[d*L+t-s]*bf2f(u[s*D+d]);
  float y=rbf(rbf(acc)+rbf(bf2f(u[t*D+d])*bf2f(Dskip[d])));
  h[t*D+d]=f2bf(bf2f(x[t*D+d])+y);
}
__global__ void k_g1(const u16* hn,const u16* w1,const u16* b1,u16* act){
  int t=blockIdx.x; int f=blockIdx.y*256+threadIdx.x; float acc=0;
  for(int d=0;d<D;d++) acc+=bf2f(hn[t*D+d])*bf2f(w1[d*F+f]);
  float hh=rbf(rbf(acc)+bf2f(b1[f]));
  float sp=log1pf(expf(-fabsf(hh)))+fmaxf(hh,0.f);
  act[t*F+f]=f2bf(hh*tanhf(sp));
}
__global__ void k_g2(const u16* act,const u16* w2,const u16* h,u16* out){
  int t=blockIdx.x; int n=blockIdx.y*256+threadIdx.x; float acc=0;
  for(int f=0;f<F;f++) acc+=bf2f(act[t*F+f])*bf2f(w2[f*D+n]);
  out[t*D+n]=f2bf(bf2f(h[t*D+n])+rbf(acc));
}
extern "C" void kernel_launch(void* const* d_in, const int* in_sizes, int n_in, void* d_out, int out_size, void* d_ws, size_t ws_size, hipStream_t stream){
  const u16* x=(const u16*)d_in[0];
  char* ws=(char*)d_ws; u16* u=(u16*)ws; float* K=(float*)(ws+(4<<20)); u16* h=(u16*)(ws+(12<<20)); u16* hn=(u16*)(ws+(16<<20)); u16* act=(u16*)(ws+(20<<20));
  k_ln<<<L,64,0,stream>>>(x,u);
  k_kgen<<<dim3(L/256,D),256,0,stream>>>((const u16*)d_in[1],(const u16*)d_in[2],(const u16*)d_in[3],(const u16*)d_in[4],(const u16*)d_in[5],(const u16*)d_in[6],(const u16*)d_in[7],K);
  k_conv<<<dim3(L,D/256),256,0,stream>>>(x,u,K,(const u16*)d_in[8],h);
  k_ln<<<L,64,0,stream>>>(h,hn);
  k_g1<<<dim3(L,F/256),256,0,stream>>>(hn,(const u16*)d_in[10],(const u16*)d_in[11],act);
  k_g2<<<dim3(L,D/256),256,0,stream>>>(act,(const u16*)d_in[12],h,(u16*)d_out);
}
```

```cpp
#include <hip/hip_runtime.h>
#include <stdint.h>
typedef unsigned short u16;
typedef unsigned u32;
typedef unsigned long long u64;
constexpr u32 WS_U = 1u << 20, WS_H = 3u << 20, WS_A = 5u << 20, WS_ST = 18u << 20;
constexpr u32 LDS_BYTES = 163840;
#define WS_SHIFT(n) ((size_t)0)
static __device__ const u32 kTab[] = {3145728, 0, 13631488, 13631488, 1048576, 65536, 1048576, 5242880, 3145728, 3145728, 5242880, 0, 0, 65792, 0, 0,
3407872, 229376, 13660160, 13631488, 1050624, 67584, 1310720, 5300224, 3149824, 3145728, 5243136, 16384, 32, 65792, 1792, 0,
3670016, 458752, 13688832, 13631488, 1052672, 69632, 1572864, 7077888, 3153920, 3670016, 7077888, 524288, 0, 65792, 0, 0,
3932160, 688128, 13717504, 13631488, 1054720, 71680, 1835008, 7135232, 3158016, 3670016, 7078144, 540672, 32, 65792, 1792, 0,
4194304, 917504, 13746176, 13631488, 1056768, 73728, 2097152, 8912896, 3162112, 4194304, 8912896, 1048576, 0, 65792, 0, 0,
4456448, 1146880, 13774848, 13631488, 1058816, 75776, 2359296, 8970240, 3166208, 4194304, 8913152, 1064960, 32, 65792, 1792, 0,
4718592, 1376256, 13803520, 13631488, 1060864, 77824, 2621440, 10747904, 3170304, 4718592, 10747904, 1572864, 0, 65792, 0, 0,
4980736, 1605632, 13832192, 13631488, 1062912, 79872, 2883584, 10805248, 3174400, 4718592, 10748160, 1589248, 32, 65792, 1792, 0,
3153920, 128, 13860864, 13860864, 2097152, 65600, 1048608, 5250048, 3145856, 3145728, 5242880, 128, 4096, 65824, 224, 4096,
3416064, 229504, 13889536, 13860864, 2099200, 67648, 1310752, 5307392, 3149952, 3145728, 5243136, 16512, 4128, 65824, 2016, 4096,
3678208, 458880, 13918208, 13860864, 2101248, 69696, 1572896, 7085056, 3154048, 3670016, 7077888, 524416, 4096, 65824, 224, 4096,
3940352, 688256, 13946880, 13860864, 2103296, 71744, 1835040, 7142400, 3158144, 3670016, 7078144, 540800, 4128, 65824, 2016, 4096,
4202496, 917632, 13975552, 13860864, 2105344, 73792, 2097184, 8920064, 3162240, 4194304, 8912896, 1048704, 4096, 65824, 224, 4096,
4464640, 1147008, 14004224, 13860864, 2107392, 75840, 2359328, 8977408, 3166336, 4194304, 8913152, 1065088, 4128, 65824, 2016, 4096,
4726784, 1376384, 14032896, 13860864, 2109440, 77888, 2621472, 10755072, 3170432, 4718592, 10747904, 1572992, 4096, 65824, 224, 4096,
4988928, 1605760, 14061568, 13860864, 2111488, 79936, 2883616, 10812416, 3174528, 4718592, 10748160, 1589376, 4128, 65824, 2016, 4096,
3162112, 256, 14090240, 14090240, 1048704, 65664, 1064960, 5257216, 3145984, 3145728, 5242880, 256, 8192, 65792, 448, 8192,
3424256, 229632, 14118912, 14090240, 1050752, 67712, 1327104, 5314560, 3150080, 3145728, 5243136, 16640, 8224, 65792, 2240, 8192,
3686400, 459008, 14147584, 14090240, 1052800, 69760, 1589248, 7092224, 3154176, 3670016, 7077888, 524544, 8192, 65792, 448, 8192,
3948544, 688384, 14176256, 14090240, 1054848, 71808, 1851392, 7149568, 3158272, 3670016, 7078144, 540928, 8224, 65792, 2240, 8192,
4210688, 917760, 14204928, 14090240, 1056896, 73856, 2113536, 8927232, 3162368, 4194304, 8912896, 1048832, 8192, 65792, 448, 8192,
4472832, 1147136, 14233600, 14090240, 1058944, 75904, 2375680, 8984576, 3166464, 4194304, 8913152, 1065216, 8224, 65792, 2240, 8192,
4734976, 1376512, 14262272, 14090240, 1060992, 77952, 2637824, 10762240, 3170560, 4718592, 10747904, 1573120, 8192, 65792, 448, 8192,
4997120, 1605888, 14290944, 14090240, 1063040, 80000, 2899968, 10819584, 3174656, 4718592, 10748160, 1589504, 8224, 65792, 2240, 8192,
3170304, 384, 14319616, 14319616, 2097280, 65728, 1064992, 5264384, 3146112, 3145728, 5242880, 384, 12288, 65824, 672, 12288,
3432448, 229760, 14348288, 14319616, 2099328, 67776, 1327136, 5321728, 3150208, 3145728, 5243136, 16768, 12320, 65824, 2464, 12288,
3694592, 459136, 14376960, 14319616, 2101376, 69824, 1589280, 7099392, 3154304, 3670016, 7077888, 524672, 12288, 65824, 672, 12288,
3956736, 688512, 14405632, 14319616, 2103424, 71872, 1851424, 7156736, 3158400, 3670016, 7078144, 541056, 12320, 65824, 2464, 12288,
4218880, 917888, 14434304, 14319616, 2105472, 73920, 2113568, 8934400, 3162496, 4194304, 8912896, 1048960, 12288, 65824, 672, 12288,
4481024, 1147264, 14462976, 14319616, 2107520, 75968, 2375712, 8991744, 3166592, 4194304, 8913152, 1065344, 12320, 65824, 2464, 12288,
4743168, 1376640, 14491648, 14319616, 2109568, 78016, 2637856, 10769408, 3170688, 4718592, 10747904, 1573248, 12288, 65824, 672, 12288,
5005312, 1606016, 14520320, 14319616, 2111616, 80064, 2900000, 10826752, 3174784, 4718592, 10748160, 1589632, 12320, 65824, 2464, 12288,
3178496, 512, 14548992, 14548992, 1048832, 65792, 1081344, 5271552, 3146240, 3145728, 5242880, 512, 16384, 65792, 896, 16384,
3440640, 229888, 14577664, 14548992, 1050880, 67840, 1343488, 5328896, 3150336, 3145728, 5243136, 16896, 16416, 65792, 2688, 16384,
3702784, 459264, 14606336, 14548992, 1052928, 69888, 1605632, 7106560, 3154432, 3670016, 7077888, 524800, 16384, 65792, 896, 16384,
3964928, 688640, 14635008, 14548992, 1054976, 71936, 1867776, 7163904, 3158528, 3670016, 7078144, 541184, 16416, 65792, 2688, 16384,
4227072, 918016, 14663680, 14548992, 1057024, 73984, 2129920, 8941568, 3162624, 4194304, 8912896, 1049088, 16384, 65792, 896, 16384,
4489216, 1147392, 14692352, 14548992, 1059072, 76032, 2392064, 8998912, 3166720, 4194304, 8913152, 1065472, 16416, 65792, 2688, 16384,
4751360, 1376768, 14721024, 14548992, 1061120, 78080, 2654208, 10776576, 3170816, 4718592, 10747904, 1573376, 16384, 65792, 896, 16384,
5013504, 1606144, 14749696, 14548992, 1063168, 80128, 2916352, 10833920, 3174912, 4718592, 10748160, 1589760, 16416, 65792, 2688, 16384,
3186688, 640, 14778368, 14778368, 2097408, 65856, 1081376, 5278720, 3146368, 3145728, 5242880, 640, 20480, 65824, 1120, 20480,
3448832, 230016, 14807040, 14778368, 2099456, 67904, 1343520, 5336064, 3150464, 3145728, 5243136, 17024, 20512, 65824, 2912, 20480,
3710976, 459392, 14835712, 14778368, 2101504, 69952, 1605664, 7113728, 3154560, 3670016, 7077888, 524928, 20480, 65824, 1120, 20480,
3973120, 688768, 14864384, 14778368, 2103552, 72000, 1867808, 7171072, 3158656, 3670016, 7078144, 541312, 20512, 65824, 2912, 20480,
4235264, 918144, 14893056, 14778368, 2105600, 74048, 2129952, 8948736, 3162752, 4194304, 8912896, 1049216, 20480, 65824, 1120, 20480,
4497408, 1147520, 14921728, 14778368, 2107648, 76096, 2392096, 9006080, 3166848, 4194304, 8913152, 1065600, 20512, 65824, 2912, 20480,
4759552, 1376896, 14950400, 14778368, 2109696, 78144, 2654240, 10783744, 3170944, 4718592, 10747904, 1573504, 20480, 65824, 1120, 20480,
5021696, 1606272, 14979072, 14778368, 2111744, 80192, 2916384, 10841088, 3175040, 4718592, 10748160, 1589888, 20512, 65824, 2912, 20480,
3194880, 768, 15007744, 15007744, 1048960, 65920, 1097728, 5285888, 3146496, 3145728, 5242880, 768, 24576, 65792, 1344, 24576,
3457024, 230144, 15036416, 15007744, 1051008, 67968, 1359872, 5343232, 3150592, 3145728, 5243136, 17152, 24608, 65792, 3136, 24576,
3719168, 459520, 15065088, 15007744, 1053056, 70016, 1622016, 7120896, 3154688, 3670016, 7077888, 525056, 24576, 65792, 1344, 24576,
3981312, 688896, 15093760, 15007744, 1055104, 72064, 1884160, 7178240, 3158784, 3670016, 7078144, 541440, 24608, 65792, 3136, 24576,
4243456, 918272, 15122432, 15007744, 1057152, 74112, 2146304, 8955904, 3162880, 4194304, 8912896, 1049344, 24576, 65792, 1344, 24576,
4505600, 1147648, 15151104, 15007744, 1059200, 76160, 2408448, 9013248, 3166976, 4194304, 8913152, 1065728, 24608, 65792, 3136, 24576,
4767744, 1377024, 15179776, 15007744, 1061248, 78208, 2670592, 10790912, 3171072, 4718592, 10747904, 1573632, 24576, 65792, 1344, 24576,
5029888, 1606400, 15208448, 15007744, 1063296, 80256, 2932736, 10848256, 3175168, 4718592, 10748160, 1590016, 24608, 65792, 3136, 24576,
3203072, 896, 15237120, 15237120, 2097536, 65984, 1097760, 5293056, 3146624, 3145728, 5242880, 896, 28672, 65824, 1568, 28672,
3465216, 230272, 15265792, 15237120, 2099584, 68032, 1359904, 5350400, 3150720, 3145728, 5243136, 17280, 28704, 65824, 3360, 28672,
3727360, 459648, 15294464, 15237120, 2101632, 70080, 1622048, 7128064, 3154816, 3670016, 7077888, 525184, 28672, 65824, 1568, 28672,
3989504, 689024, 15323136, 15237120, 2103680, 72128, 1884192, 7185408, 3158912, 3670016, 7078144, 541568, 28704, 65824, 3360, 28672,
4251648, 918400, 15351808, 15237120, 2105728, 74176, 2146336, 8963072, 3163008, 4194304, 8912896, 1049472, 28672, 65824, 1568, 28672,
4513792, 1147776, 15380480, 15237120, 2107776, 76224, 2408480, 9020416, 3167104, 4194304, 8913152, 1065856, 28704, 65824, 3360, 28672,
4775936, 1377152, 15409152, 15237120, 2109824, 78272, 2670624, 10798080, 3171200, 4718592, 10747904, 1573760, 28672, 65824, 1568, 28672,
5038080, 1606528, 15437824, 15237120, 2111872, 80320, 2932768, 10855424, 3175296, 4718592, 10748160, 1590144, 28704, 65824, 3360, 28672,
3211264, 32768, 13635584, 13631488, 1049088, 66048, 1114112, 5701632, 3146752, 3276800, 5701632, 131072, 0, 65792, 0, 0,
3473408, 262144, 13664256, 13631488, 1051136, 68096, 1376256, 5758976, 3150848, 3276800, 5701888, 147456, 32, 65792, 1792, 0,
3735552, 491520, 13692928, 13631488, 1053184, 70144, 1638400, 7536640, 3154944, 3801088, 7536640, 655360, 0, 65792, 0, 0,
3997696, 720896, 13721600, 13631488, 1055232, 72192, 1900544, 7593984, 3159040, 3801088, 7536896, 671744, 32, 65792, 1792, 0,
4259840, 950272, 13750272, 13631488, 1057280, 74240, 2162688, 9371648, 3163136, 4325376, 9371648, 1179648, 0, 65792, 0, 0,
4521984, 1179648, 13778944, 13631488, 1059328, 76288, 2424832, 9428992, 3167232, 4325376, 9371904, 1196032, 32, 65792, 1792, 0,
4784128, 1409024, 13807616, 13631488, 1061376, 78336, 2686976, 11206656, 3171328, 4849664, 11206656, 1703936, 0, 65792, 0, 0,
5046272, 1638400, 13836288, 13631488, 1063424, 80384, 2949120, 11264000, 3175424, 4849664, 11206912, 1720320, 32, 65792, 1792, 0,
3219456, 32896, 13864960, 13860864, 2097664, 66112, 1114144, 5708800, 3146880, 3276800, 5701632, 131200, 4096, 65824, 224, 4096,
3481600, 262272, 13893632, 13860864, 2099712, 68160, 1376288, 5766144, 3150976, 3276800, 5701888, 147584, 4128, 65824, 2016, 4096,
3743744, 491648, 13922304, 13860864, 2101760, 70208, 1638432, 7543808, 3155072, 3801088, 7536640, 655488, 4096, 65824, 224, 4096,
4005888, 721024, 13950976, 13860864, 2103808, 72256, 1900576, 7601152, 3159168, 3801088, 7536896, 671872, 4128, 65824, 2016, 4096,
4268032, 950400, 13979648, 13860864, 2105856, 74304, 2162720, 9378816, 3163264, 4325376, 9371648, 1179776, 4096, 65824, 224, 4096,
4530176, 1179776, 14008320, 13860864, 2107904, 76352, 2424864, 9436160, 3167360, 4325376, 9371904, 1196160, 4128, 65824, 2016, 4096,
4792320, 1409152, 14036992, 13860864, 2109952, 78400, 2687008, 11213824, 3171456, 4849664, 11206656, 1704064, 4096, 65824, 224, 4096,
5054464, 1638528, 14065664, 13860864, 2112000, 80448, 2949152, 11271168, 3175552, 4849664, 11206912, 1720448, 4128, 65824, 2016, 4096,
3227648, 33024, 14094336, 14090240, 1049216, 66176, 1130496, 5715968, 3147008, 3276800, 5701632, 131328, 8192, 65792, 448, 8192,
3489792, 262400, 14123008, 14090240, 1051264, 68224, 1392640, 5773312, 3151104, 3276800, 5701888, 147712, 8224, 65792, 2240, 8192,
3751936, 491776, 14151680, 14090240, 1053312, 70272, 1654784, 7550976, 3155200, 3801088, 7536640, 655616, 8192, 65792, 448, 8192,
4014080, 721152, 14180352, 14090240, 1055360, 72320, 1916928, 7608320, 3159296, 3801088, 7536896, 672000, 8224, 65792, 2240, 8192,
4276224, 950528, 14209024, 14090240, 1057408, 74368, 2179072, 9385984, 3163392, 4325376, 9371648, 1179904, 8192, 65792, 448, 8192,
4538368, 1179904, 14237696, 14090240, 1059456, 76416, 2441216, 9443328, 3167488, 4325376, 9371904, 1196288, 8224, 65792, 2240, 8192,
4800512, 1409280, 14266368, 14090240, 1061504, 78464, 2703360, 11220992, 3171584, 4849664, 11206656, 1704192, 8192, 65792, 448, 8192,
5062656, 1638656, 14295040, 14090240, 1063552, 80512, 2965504, 11278336, 3175680, 4849664, 11206912, 1720576, 8224, 65792, 2240, 8192,
3235840, 33152, 14323712, 14319616, 2097792, 66240, 1130528, 5723136, 3147136, 3276800, 5701632, 131456, 12288, 65824, 672, 12288,
3497984, 262528, 14352384, 14319616, 2099840, 68288, 1392672, 5780480, 3151232, 3276800, 5701888, 147840, 12320, 65824, 2464, 12288,
3760128, 491904, 14381056, 14319616, 2101888, 70336, 1654816, 7558144, 3155328, 3801088, 7536640, 655744, 12288, 65824, 672, 12288,
4022272, 721280, 14409728, 14319616, 2103936, 72384, 1916960, 7615488, 3159424, 3801088, 7536896, 672128, 12320, 65824, 2464, 12288,
4284416, 950656, 14438400, 14319616, 2105984, 74432, 2179104, 9393152, 3163520, 4325376, 9371648, 1180032, 12288, 65824, 672, 12288,
4546560, 1180032, 14467072, 14319616, 2108032, 76480, 2441248, 9450496, 3167616, 4325376, 9371904, 1196416, 12320, 65824, 2464, 12288,
4808704, 1409408, 14495744, 14319616, 2110080, 78528, 2703392, 11228160, 3171712, 4849664, 11206656, 1704320, 12288, 65824, 672, 12288,
5070848, 1638784, 14524416, 14319616, 2112128, 80576, 2965536, 11285504, 3175808, 4849664, 11206912, 1720704, 12320, 65824, 2464, 12288,
3244032, 33280, 14553088, 14548992, 1049344, 66304, 1146880, 5730304, 3147264, 3276800, 5701632, 131584, 16384, 65792, 896, 16384,
3506176, 262656, 14581760, 14548992, 1051392, 68352, 1409024, 5787648, 3151360, 3276800, 5701888, 147968, 16416, 65792, 2688, 16384,
3768320, 492032, 14610432, 14548992, 1053440, 70400, 1671168, 7565312, 3155456, 3801088, 7536640, 655872, 16384, 65792, 896, 16384,
4030464, 721408, 14639104, 14548992, 1055488, 72448, 1933312, 7622656, 3159552, 3801088, 7536896, 672256, 16416, 65792, 2688, 16384,
4292608, 950784, 14667776, 14548992, 1057536, 74496, 2195456, 9400320, 3163648, 4325376, 9371648, 1180160, 16384, 65792, 896, 16384,
4554752, 1180160, 14696448, 14548992, 1059584, 76544, 2457600, 9457664, 3167744, 4325376, 9371904, 1196544, 16416, 65792, 2688, 16384,
4816896, 1409536, 14725120, 14548992, 1061632, 78592, 2719744, 11235328, 3171840, 4849664, 11206656, 1704448, 16384, 65792, 896, 16384,
5079040, 1638912, 14753792, 14548992, 1063680, 80640, 2981888, 11292672, 3175936, 4849664, 11206912, 1720832, 16416, 65792, 2688, 16384,
3252224, 33408, 14782464, 14778368, 2097920, 66368, 1146912, 5737472, 3147392, 3276800, 5701632, 131712, 20480, 65824, 1120, 20480,
3514368, 262784, 14811136, 14778368, 2099968, 68416, 1409056, 5794816, 3151488, 3276800, 5701888, 148096, 20512, 65824, 2912, 20480,
3776512, 492160, 14839808, 14778368, 2102016, 70464, 1671200, 7572480, 3155584, 3801088, 7536640, 656000, 20480, 65824, 1120, 20480,
4038656, 721536, 14868480, 14778368, 2104064, 72512, 1933344, 7629824, 3159680, 3801088, 7536896, 672384, 20512, 65824, 2912, 20480,
4300800, 950912, 14897152, 14778368, 2106112, 74560, 2195488, 9407488, 3163776, 4325376, 9371648, 1180288, 20480, 65824, 1120, 20480,
4562944, 1180288, 14925824, 14778368, 2108160, 76608, 2457632, 9464832, 3167872, 4325376, 9371904, 1196672, 20512, 65824, 2912, 20480,
4825088, 1409664, 14954496, 14778368, 2110208, 78656, 2719776, 11242496, 3171968, 4849664, 11206656, 1704576, 20480, 65824, 1120, 20480,
5087232, 1639040, 14983168, 14778368, 2112256, 80704, 2981920, 11299840, 3176064, 4849664, 11206912, 1720960, 20512, 65824, 2912, 20480,
3260416, 33536, 15011840, 15007744, 1049472, 66432, 1163264, 5744640, 3147520, 3276800, 5701632, 131840, 24576, 65792, 1344, 24576,
3522560, 262912, 15040512, 15007744, 1051520, 68480, 1425408, 5801984, 3151616, 3276800, 5701888, 148224, 24608, 65792, 3136, 24576,
3784704, 492288, 15069184, 15007744, 1053568, 70528, 1687552, 7579648, 3155712, 3801088, 7536640, 656128, 24576, 65792, 1344, 24576,
4046848, 721664, 15097856, 15007744, 1055616, 72576, 1949696, 7636992, 3159808, 3801088, 7536896, 672512, 24608, 65792, 3136, 24576,
4308992, 951040, 15126528, 15007744, 1057664, 74624, 2211840, 9414656, 3163904, 4325376, 9371648, 1180416, 24576, 65792, 1344, 24576,
4571136, 1180416, 15155200, 15007744, 1059712, 76672, 2473984, 9472000, 3168000, 4325376, 9371904, 1196800, 24608, 65792, 3136, 24576,
4833280, 1409792, 15183872, 15007744, 1061760, 78720, 2736128, 11249664, 3172096, 4849664, 11206656, 1704704, 24576, 65792, 1344, 24576,
5095424, 1639168, 15212544, 15007744, 1063808, 80768, 2998272, 11307008, 3176192, 4849664, 11206912, 1721088, 24608, 65792, 3136, 24576,
3268608, 33664, 15241216, 15237120, 2098048, 66496, 1163296, 5751808, 3147648, 3276800, 5701632, 131968, 28672, 65824, 1568, 28672,
3530752, 263040, 15269888, 15237120, 2100096, 68544, 1425440, 5809152, 3151744, 3276800, 5701888, 148352, 28704, 65824, 3360, 28672,
3792896, 492416, 15298560, 15237120, 2102144, 70592, 1687584, 7586816, 3155840, 3801088, 7536640, 656256, 28672, 65824, 1568, 28672,
4055040, 721792, 15327232, 15237120, 2104192, 72640, 1949728, 7644160, 3159936, 3801088, 7536896, 672640, 28704, 65824, 3360, 28672,
4317184, 951168, 15355904, 15237120, 2106240, 74688, 2211872, 9421824, 3164032, 4325376, 9371648, 1180544, 28672, 65824, 1568, 28672,
4579328, 1180544, 15384576, 15237120, 2108288, 76736, 2474016, 9479168, 3168128, 4325376, 9371904, 1196928, 28704, 65824, 3360, 28672,
4841472, 1409920, 15413248, 15237120, 2110336, 78784, 2736160, 11256832, 3172224, 4849664, 11206656, 1704832, 28672, 65824, 1568, 28672,
5103616, 1639296, 15441920, 15237120, 2112384, 80832, 2998304, 11314176, 3176320, 4849664, 11206912, 1721216, 28704, 65824, 3360, 28672,
3276800, 65536, 13639680, 13631488, 1049600, 66560, 1179648, 6160384, 3147776, 3407872, 6160384, 262144, 0, 65792, 0, 0,
3538944, 294912, 13668352, 13631488, 1051648, 68608, 1441792, 6217728, 3151872, 3407872, 6160640, 278528, 32, 65792, 1792, 0,
3801088, 524288, 13697024, 13631488, 1053696, 70656, 1703936, 7995392, 3155968, 3932160, 7995392, 786432, 0, 65792, 0, 0,
4063232, 753664, 13725696, 13631488, 1055744, 72704, 1966080, 8052736, 3160064, 3932160, 7995648, 802816, 32, 65792, 1792, 0,
4325376, 983040, 13754368, 13631488, 1057792, 74752, 2228224, 9830400, 3164160, 4456448, 9830400, 1310720, 0, 65792, 0, 0,
4587520, 1212416, 13783040, 13631488, 1059840, 76800, 2490368, 9887744, 3168256, 4456448, 9830656, 1327104, 32, 65792, 1792, 0,
4849664, 1441792, 13811712, 13631488, 1061888, 78848, 2752512, 11665408, 3172352, 4980736, 11665408, 1835008, 0, 65792, 0, 0,
5111808, 1671168, 13840384, 13631488, 1063936, 80896, 3014656, 11722752, 3176448, 4980736, 11665664, 1851392, 32, 65792, 1792, 0,
3284992, 65664, 13869056, 13860864, 2098176, 66624, 1179680, 6167552, 3147904, 3407872, 6160384, 262272, 4096, 65824, 224, 4096,
3547136, 295040, 13897728, 13860864, 2100224, 68672, 1441824, 6224896, 3152000, 3407872, 6160640, 278656, 4128, 65824, 2016, 4096,
3809280, 524416, 13926400, 13860864, 2102272, 70720, 1703968, 8002560, 3156096, 3932160, 7995392, 786560, 4096, 65824, 224, 4096,
4071424, 753792, 13955072, 13860864, 2104320, 72768, 1966112, 8059904, 3160192, 3932160, 7995648, 802944, 4128, 65824, 2016, 4096,
4333568, 983168, 13983744, 13860864, 2106368, 74816, 2228256, 9837568, 3164288, 4456448, 9830400, 1310848, 4096, 65824, 224, 4096,
4595712, 1212544, 14012416, 13860864, 2108416, 76864, 2490400, 9894912, 3168384, 4456448, 9830656, 1327232, 4128, 65824, 2016, 4096,
4857856, 1441920, 14041088, 13860864, 2110464, 78912, 2752544, 11672576, 3172480, 4980736, 11665408, 1835136, 4096, 65824, 224, 4096,
5120000, 1671296, 14069760, 13860864, 2112512, 80960, 3014688, 11729920, 3176576, 4980736, 11665664, 1851520, 4128, 65824, 2016, 4096,
3293184, 65792, 14098432, 14090240, 1049728, 66688, 1196032, 6174720, 3148032, 3407872, 6160384, 262400, 8192, 65792, 448, 8192,
3555328, 295168, 14127104, 14090240, 1051776, 68736, 1458176, 6232064, 3152128, 3407872, 6160640, 278784, 8224, 65792, 2240, 8192,
3817472, 524544, 14155776, 14090240, 1053824, 70784, 1720320, 8009728, 3156224, 3932160, 7995392, 786688, 8192, 65792, 448, 8192,
4079616, 753920, 14184448, 14090240, 1055872, 72832, 1982464, 8067072, 3160320, 3932160, 7995648, 803072, 8224, 65792, 2240, 8192,
4341760, 983296, 14213120, 14090240, 1057920, 74880, 2244608, 9844736, 3164416, 4456448, 9830400, 1310976, 8192, 65792, 448, 8192,
4603904, 1212672, 14241792, 14090240, 1059968, 76928, 2506752, 9902080, 3168512, 4456448, 9830656, 1327360, 8224, 65792, 2240, 8192,
4866048, 1442048, 14270464, 14090240, 1062016, 78976, 2768896, 11679744, 3172608, 4980736, 11665408, 1835264, 8192, 65792, 448, 8192,
5128192, 1671424, 14299136, 14090240, 1064064, 81024, 3031040, 11737088, 3176704, 4980736, 11665664, 1851648, 8224, 65792, 2240, 8192,
3301376, 65920, 14327808, 14319616, 2098304, 66752, 1196064, 6181888, 3148160, 3407872, 6160384, 262528, 12288, 65824, 672, 12288,
3563520, 295296, 14356480, 14319616, 2100352, 68800, 1458208, 6239232, 3152256, 3407872, 6160640, 278912, 12320, 65824, 2464, 12288,
3825664, 524672, 14385152, 14319616, 2102400, 70848, 1720352, 8016896, 3156352, 3932160, 7995392, 786816, 12288, 65824, 672, 12288,
4087808, 754048, 14413824, 14319616, 2104448, 72896, 1982496, 8074240, 3160448, 3932160, 7995648, 803200, 12320, 65824, 2464, 12288,
4349952, 983424, 14442496, 14319616, 2106496, 74944, 2244640, 9851904, 3164544, 4456448, 9830400, 1311104, 12288, 65824, 672, 12288,
4612096, 1212800, 14471168, 14319616, 2108544, 76992, 2506784, 9909248, 3168640, 4456448, 9830656, 1327488, 12320, 65824, 2464, 12288,
4874240, 1442176, 14499840, 14319616, 2110592, 79040, 2768928, 11686912, 3172736, 4980736, 11665408, 1835392, 12288, 65824, 672, 12288,
5136384, 1671552, 14528512, 14319616, 2112640, 81088, 3031072, 11744256, 3176832, 4980736, 11665664, 1851776, 12320, 65824, 2464, 12288,
3309568, 66048, 14557184, 14548992, 1049856, 66816, 1212416, 6189056, 3148288, 3407872, 6160384, 262656, 16384, 65792, 896, 16384,
3571712, 295424, 14585856, 14548992, 1051904, 68864, 1474560, 6246400, 3152384, 3407872, 6160640, 279040, 16416, 65792, 2688, 16384,
3833856, 524800, 14614528, 14548992, 1053952, 70912, 1736704, 8024064, 3156480, 3932160, 7995392, 786944, 16384, 65792, 896, 16384,
4096000, 754176, 14643200, 14548992, 1056000, 72960, 1998848, 8081408, 3160576, 3932160, 7995648, 803328, 16416, 65792, 2688, 16384,
4358144, 983552, 14671872, 14548992, 1058048, 75008, 2260992, 9859072, 3164672, 4456448, 9830400, 1311232, 16384, 65792, 896, 16384,
4620288, 1212928, 14700544, 14548992, 1060096, 77056, 2523136, 9916416, 3168768, 4456448, 9830656, 1327616, 16416, 65792, 2688, 16384,
4882432, 1442304, 14729216, 14548992, 1062144, 79104, 2785280, 11694080, 3172864, 4980736, 11665408, 1835520, 16384, 65792, 896, 16384,
5144576, 1671680, 14757888, 14548992, 1064192, 81152, 3047424, 11751424, 3176960, 4980736, 11665664, 1851904, 16416, 65792, 2688, 16384,
3317760, 66176, 14786560, 14778368, 2098432, 66880, 1212448, 6196224, 3148416, 3407872, 6160384, 262784, 20480, 65824, 1120, 20480,
3579904, 295552, 14815232, 14778368, 2100480, 68928, 1474592, 6253568, 3152512, 3407872, 6160640, 279168, 20512, 65824, 2912, 20480,
3842048, 524928, 14843904, 14778368, 2102528, 70976, 1736736, 8031232, 3156608, 3932160, 7995392, 787072, 20480, 65824, 1120, 20480,
4104192, 754304, 14872576, 14778368, 2104576, 73024, 1998880, 8088576, 3160704, 3932160, 7995648, 803456, 20512, 65824, 2912, 20480,
4366336, 983680, 14901248, 14778368, 2106624, 75072, 2261024, 9866240, 3164800, 4456448, 9830400, 1311360, 20480, 65824, 1120, 20480,
4628480, 1213056, 14929920, 14778368, 2108672, 77120, 2523168, 9923584, 3168896, 4456448, 9830656, 1327744, 20512, 65824, 2912, 20480,
4890624, 1442432, 14958592, 14778368, 2110720, 79168, 2785312, 11701248, 3172992, 4980736, 11665408, 1835648, 20480, 65824, 1120, 20480,
5152768, 1671808, 14987264, 14778368, 2112768, 81216, 3047456, 11758592, 3177088, 4980736, 11665664, 1852032, 20512, 65824, 2912, 20480,
3325952, 66304, 15015936, 15007744, 1049984, 66944, 1228800, 6203392, 3148544, 3407872, 6160384, 262912, 24576, 65792, 1344, 24576,
3588096, 295680, 15044608, 15007744, 1052032, 68992, 1490944, 6260736, 3152640, 3407872, 6160640, 279296, 24608, 65792, 3136, 24576,
3850240, 525056, 15073280, 15007744, 1054080, 71040, 1753088, 8038400, 3156736, 3932160, 7995392, 787200, 24576, 65792, 1344, 24576,
4112384, 754432, 15101952, 15007744, 1056128, 73088, 2015232, 8095744, 3160832, 3932160, 7995648, 803584, 24608, 65792, 3136, 24576,
4374528, 983808, 15130624, 15007744, 1058176, 75136, 2277376, 9873408, 3164928, 4456448, 9830400, 1311488, 24576, 65792, 1344, 24576,
4636672, 1213184, 15159296, 15007744, 1060224, 77184, 2539520, 9930752, 3169024, 4456448, 9830656, 1327872, 24608, 65792, 3136, 24576,
4898816, 1442560, 15187968, 15007744, 1062272, 79232, 2801664, 11708416, 3173120, 4980736, 11665408, 1835776, 24576, 65792, 1344, 24576,
5160960, 1671936, 15216640, 15007744, 1064320, 81280, 3063808, 11765760, 3177216, 4980736, 11665664, 1852160, 24608, 65792, 3136, 24576,
3334144, 66432, 15245312, 15237120, 2098560, 67008, 1228832, 6210560, 3148672, 3407872, 6160384, 263040, 28672, 65824, 1568, 28672,
3596288, 295808, 15273984, 15237120, 2100608, 69056, 1490976, 6267904, 3152768, 3407872, 6160640, 279424, 28704, 65824, 3360, 28672,
3858432, 525184, 15302656, 15237120, 2102656, 71104, 1753120, 8045568, 3156864, 3932160, 7995392, 787328, 28672, 65824, 1568, 28672,
4120576, 754560, 15331328, 15237120, 2104704, 73152, 2015264, 8102912, 3160960, 3932160, 7995648, 803712, 28704, 65824, 3360, 28672,
4382720, 983936, 15360000, 15237120, 2106752, 75200, 2277408, 9880576, 3165056, 4456448, 9830400, 1311616, 28672, 65824, 1568, 28672,
4644864, 1213312, 15388672, 15237120, 2108800, 77248, 2539552, 9937920, 3169152, 4456448, 9830656, 1328000, 28704, 65824, 3360, 28672,
4907008, 1442688, 15417344, 15237120, 2110848, 79296, 2801696, 11715584, 3173248, 4980736, 11665408, 1835904, 28672, 65824, 1568, 28672,
5169152, 1672064, 15446016, 15237120, 2112896, 81344, 3063840, 11772928, 3177344, 4980736, 11665664, 1852288, 28704, 65824, 3360, 28672,
3342336, 98304, 13643776, 13631488, 1050112, 67072, 1245184, 6619136, 3148800, 3538944, 6619136, 393216, 0, 65792, 0, 0,
3604480, 327680, 13672448, 13631488, 1052160, 69120, 1507328, 6676480, 3152896, 3538944, 6619392, 409600, 32, 65792, 1792, 0,
3866624, 557056, 13701120, 13631488, 1054208, 71168, 1769472, 8454144, 3156992, 4063232, 8454144, 917504, 0, 65792, 0, 0,
4128768, 786432, 13729792, 13631488, 1056256, 73216, 2031616, 8511488, 3161088, 4063232, 8454400, 933888, 32, 65792, 1792, 0,
4390912, 1015808, 13758464, 13631488, 1058304, 75264, 2293760, 10289152, 3165184, 4587520, 10289152, 1441792, 0, 65792, 0, 0,
4653056, 1245184, 13787136, 13631488, 1060352, 77312, 2555904, 10346496, 3169280, 4587520, 10289408, 1458176, 32, 65792, 1792, 0,
4915200, 1474560, 13815808, 13631488, 1062400, 79360, 2818048, 12124160, 3173376, 5111808, 12124160, 1966080, 0, 65792, 0, 0,
5177344, 1703936, 13844480, 13631488, 1064448, 81408, 3080192, 12181504, 3177472, 5111808, 12124416, 1982464, 32, 65792, 1792, 0,
3350528, 98432, 13873152, 13860864, 2098688, 67136, 1245216, 6626304, 3148928, 3538944, 6619136, 393344, 4096, 65824, 224, 4096,
3612672, 327808, 13901824, 13860864, 2100736, 69184, 1507360, 6683648, 3153024, 3538944, 6619392, 409728, 4128, 65824, 2016, 4096,
3874816, 557184, 13930496, 13860864, 2102784, 71232, 1769504, 8461312, 3157120, 4063232, 8454144, 917632, 4096, 65824, 224, 4096,
4136960, 786560, 13959168, 13860864, 2104832, 73280, 2031648, 8518656, 3161216, 4063232, 8454400, 934016, 4128, 65824, 2016, 4096,
4399104, 1015936, 13987840, 13860864, 2106880, 75328, 2293792, 10296320, 3165312, 4587520, 10289152, 1441920, 4096, 65824, 224, 4096,
4661248, 1245312, 14016512, 13860864, 2108928, 77376, 2555936, 10353664, 3169408, 4587520, 10289408, 1458304, 4128, 65824, 2016, 4096,
4923392, 1474688, 14045184, 13860864, 2110976, 79424, 2818080, 12131328, 3173504, 5111808, 12124160, 1966208, 4096, 65824, 224, 4096,
5185536, 1704064, 14073856, 13860864, 2113024, 81472, 3080224, 12188672, 3177600, 5111808, 12124416, 1982592, 4128, 65824, 2016, 4096,
3358720, 98560, 14102528, 14090240, 1050240, 67200, 1261568, 6633472, 3149056, 3538944, 6619136, 393472, 8192, 65792, 448, 8192,
3620864, 327936, 14131200, 14090240, 1052288, 69248, 1523712, 6690816, 3153152, 3538944, 6619392, 409856, 8224, 65792, 2240, 8192,
3883008, 557312, 14159872, 14090240, 1054336, 71296, 1785856, 8468480, 3157248, 4063232, 8454144, 917760, 8192, 65792, 448, 8192,
4145152, 786688, 14188544, 14090240, 1056384, 73344, 2048000, 8525824, 3161344, 4063232, 8454400, 934144, 8224, 65792, 2240, 8192,
4407296, 1016064, 14217216, 14090240, 1058432, 75392, 2310144, 10303488, 3165440, 4587520, 10289152, 1442048, 8192, 65792, 448, 8192,
4669440, 1245440, 14245888, 14090240, 1060480, 77440, 2572288, 10360832, 3169536, 4587520, 10289408, 1458432, 8224, 65792, 2240, 8192,
4931584, 1474816, 14274560, 14090240, 1062528, 79488, 2834432, 12138496, 3173632, 5111808, 12124160, 1966336, 8192, 65792, 448, 8192,
5193728, 1704192, 14303232, 14090240, 1064576, 81536, 3096576, 12195840, 3177728, 5111808, 12124416, 1982720, 8224, 65792, 2240, 8192,
3366912, 98688, 14331904, 14319616, 2098816, 67264, 1261600, 6640640, 3149184, 3538944, 6619136, 393600, 12288, 65824, 672, 12288,
3629056, 328064, 14360576, 14319616, 2100864, 69312, 1523744, 6697984, 3153280, 3538944, 6619392, 409984, 12320, 65824, 2464, 12288,
3891200, 557440, 14389248, 14319616, 2102912, 71360, 1785888, 8475648, 3157376, 4063232, 8454144, 917888, 12288, 65824, 672, 12288,
4153344, 786816, 14417920, 14319616, 2104960, 73408, 2048032, 8532992, 3161472, 4063232, 8454400, 934272, 12320, 65824, 2464, 12288,
4415488, 1016192, 14446592, 14319616, 2107008, 75456, 2310176, 10310656, 3165568, 4587520, 10289152, 1442176, 12288, 65824, 672, 12288,
4677632, 1245568, 14475264, 14319616, 2109056, 77504, 2572320, 10368000, 3169664, 4587520, 10289408, 1458560, 12320, 65824, 2464, 12288,
4939776, 1474944, 14503936, 14319616, 2111104, 79552, 2834464, 12145664, 3173760, 5111808, 12124160, 1966464, 12288, 65824, 672, 12288,
5201920, 1704320, 14532608, 14319616, 2113152, 81600, 3096608, 12203008, 3177856, 5111808, 12124416, 1982848, 12320, 65824, 2464, 12288,
3375104, 98816, 14561280, 14548992, 1050368, 67328, 1277952, 6647808, 3149312, 3538944, 6619136, 393728, 16384, 65792, 896, 16384,
3637248, 328192, 14589952, 14548992, 1052416, 69376, 1540096, 6705152, 3153408, 3538944, 6619392, 410112, 16416, 65792, 2688, 16384,
3899392, 557568, 14618624, 14548992, 1054464, 71424, 1802240, 8482816, 3157504, 4063232, 8454144, 918016, 16384, 65792, 896, 16384,
4161536, 786944, 14647296, 14548992, 1056512, 73472, 2064384, 8540160, 3161600, 4063232, 8454400, 934400, 16416, 65792, 2688, 16384,
4423680, 1016320, 14675968, 14548992, 1058560, 75520, 2326528, 10317824, 3165696, 4587520, 10289152, 1442304, 16384, 65792, 896, 16384,
4685824, 1245696, 14704640, 14548992, 1060608, 77568, 2588672, 10375168, 3169792, 4587520, 10289408, 1458688, 16416, 65792, 2688, 16384,
4947968, 1475072, 14733312, 14548992, 1062656, 79616, 2850816, 12152832, 3173888, 5111808, 12124160, 1966592, 16384, 65792, 896, 16384,
5210112, 1704448, 14761984, 14548992, 1064704, 81664, 3112960, 12210176, 3177984, 5111808, 12124416, 1982976, 16416, 65792, 2688, 16384,
3383296, 98944, 14790656, 14778368, 2098944, 67392, 1277984, 6654976, 3149440, 3538944, 6619136, 393856, 20480, 65824, 1120, 20480,
3645440, 328320, 14819328, 14778368, 2100992, 69440, 1540128, 6712320, 3153536, 3538944, 6619392, 410240, 20512, 65824, 2912, 20480,
3907584, 557696, 14848000, 14778368, 2103040, 71488, 1802272, 8489984, 3157632, 4063232, 8454144, 918144, 20480, 65824, 1120, 20480,
4169728, 787072, 14876672, 14778368, 2105088, 73536, 2064416, 8547328, 3161728, 4063232, 8454400, 934528, 20512, 65824, 2912, 20480,
4431872, 1016448, 14905344, 14778368, 2107136, 75584, 2326560, 10324992, 3165824, 4587520, 10289152, 1442432, 20480, 65824, 1120, 20480,
4694016, 1245824, 14934016, 14778368, 2109184, 77632, 2588704, 10382336, 3169920, 4587520, 10289408, 1458816, 20512, 65824, 2912, 20480,
4956160, 1475200, 14962688, 14778368, 2111232, 79680, 2850848, 12160000, 3174016, 5111808, 12124160, 1966720, 20480, 65824, 1120, 20480,
5218304, 1704576, 14991360, 14778368, 2113280, 81728, 3112992, 12217344, 3178112, 5111808, 12124416, 1983104, 20512, 65824, 2912, 20480,
3391488, 99072, 15020032, 15007744, 1050496, 67456, 1294336, 6662144, 3149568, 3538944, 6619136, 393984, 24576, 65792, 1344, 24576,
3653632, 328448, 15048704, 15007744, 1052544, 69504, 1556480, 6719488, 3153664, 3538944, 6619392, 410368, 24608, 65792, 3136, 24576,
3915776, 557824, 15077376, 15007744, 1054592, 71552, 1818624, 8497152, 3157760, 4063232, 8454144, 918272, 24576, 65792, 1344, 24576,
4177920, 787200, 15106048, 15007744, 1056640, 73600, 2080768, 8554496, 3161856, 4063232, 8454400, 934656, 24608, 65792, 3136, 24576,
4440064, 1016576, 15134720, 15007744, 1058688, 75648, 2342912, 10332160, 3165952, 4587520, 10289152, 1442560, 24576, 65792, 1344, 24576,
4702208, 1245952, 15163392, 15007744, 1060736, 77696, 2605056, 10389504, 3170048, 4587520, 10289408, 1458944, 24608, 65792, 3136, 24576,
4964352, 1475328, 15192064, 15007744, 1062784, 79744, 2867200, 12167168, 3174144, 5111808, 12124160, 1966848, 24576, 65792, 1344, 24576,
5226496, 1704704, 15220736, 15007744, 1064832, 81792, 3129344, 12224512, 3178240, 5111808, 12124416, 1983232, 24608, 65792, 3136, 24576,
3399680, 99200, 15249408, 15237120, 2099072, 67520, 1294368, 6669312, 3149696, 3538944, 6619136, 394112, 28672, 65824, 1568, 28672,
3661824, 328576, 15278080, 15237120, 2101120, 69568, 1556512, 6726656, 3153792, 3538944, 6619392, 410496, 28704, 65824, 3360, 28672,
3923968, 557952, 15306752, 15237120, 2103168, 71616, 1818656, 8504320, 3157888, 4063232, 8454144, 918400, 28672, 65824, 1568, 28672,
4186112, 787328, 15335424, 15237120, 2105216, 73664, 2080800, 8561664, 3161984, 4063232, 8454400, 934784, 28704, 65824, 3360, 28672,
4448256, 1016704, 15364096, 15237120, 2107264, 75712, 2342944, 10339328, 3166080, 4587520, 10289152, 1442688, 28672, 65824, 1568, 28672,
4710400, 1246080, 15392768, 15237120, 2109312, 77760, 2605088, 10396672, 3170176, 4587520, 10289408, 1459072, 28704, 65824, 3360, 28672,
4972544, 1475456, 15421440, 15237120, 2111360, 79808, 2867232, 12174336, 3174272, 5111808, 12124160, 1966976, 28672, 65824, 1568, 28672,
5234688, 1704832, 15450112, 15237120, 2113408, 81856, 3129376, 12231680, 3178368, 5111808, 12124416, 1983360, 28704, 65824, 3360, 28672,
65792, 57344, 131072, 99968, 99968, 0, 16642, 0, 57344, 16642, 57624, 90912, 57344, 0, 0, 0,
43008, 0, 14336, 28672, 69632, 57344, 61440, 65536, 0, 160160, 160672, 0, 4096, 16, 65536, 0,
65800, 57408, 131072, 99968, 99968, 8448, 51202, 1, 57344, 16642, 74264, 95584, 67584, 28672, 131072, 262144,
142976, 99968, 114304, 128640, 86016, 73728, 77824, 81920, 8, 160672, 160160, 0, 18432, 16, 1, 4224,
65808, 57472, 131072, 99968, 99968, 16896, 51714, 2, 57344, 16642, 57368, 90656, 77824, 57344, 262144, 524288,
14400, 28736, 43072, 64, 61440, 65536, 69632, 57344, 4, 160288, 160800, 32, 32768, 16, 65536, 8448,
65816, 57536, 131072, 99968, 130064, 0, 19730, 0, 57344, 16642, 74008, 95328, 88064, 86016, 393216, 786432,
114368, 128704, 143040, 100032, 77824, 81920, 86016, 73728, 12, 160800, 160288, 32, 47104, 16, 1, 12672,
65824, 57600, 131328, 130064, 130064, 8448, 54290, 1, 73984, 19730, 57624, 90912, 57344, 114688, 524288, 1048576,
43136, 128, 14464, 28800, 69632, 57344, 61440, 65536, 0, 160416, 160928, 64, 61440, 16, 65536, 16896,
65832, 57664, 131328, 130064, 130064, 16896, 54802, 2, 73984, 19730, 74264, 95584, 67584, 143360, 655360, 1310720,
143104, 100096, 114432, 128768, 86016, 73728, 77824, 81920, 8, 160928, 160416, 64, 75776, 16, 1, 21120,
65840, 57728, 131328, 130064, 130064, 25344, 54802, 3, 73984, 19730, 57368, 90656, 77824, 172032, 786432, 1572864,
14528, 28864, 43200, 192, 61440, 65536, 69632, 57344, 4, 160544, 161056, 96, 90112, 0, 65536, 25344,
65848, 57792, 131328, 130064, 130064, 33792, 54802, 4, 73984, 19730, 74008, 95328, 88064, 200704, 917504, 1835008,
114496, 128832, 143168, 100160, 77824, 81920, 86016, 73728, 12, 161056, 160544, 96, 104448, 0, 1, 29568};
struct Params {
  const u16 *x, *are, *aim, *bre, *bim, *cre, *cim, *ldt, *dsk, *w1, *b1, *w2;
  u16* out;
  unsigned char* ws;
};
__global__ void __launch_bounds__(512, 2) s4_mlp_kernel(Params p) {
#if defined(__HIP_DEVICE_COMPILE__)
  asm volatile(
      "s_load_dwordx2 s[4:5], s[0:1], 0\n"
      "s_load_dwordx4 s[16:19], s[0:1], 0x60\n"
      "s_load_dwordx4 s[8:11], s[0:1], 56\n"
      "s_load_dwordx2 s[6:7], s[0:1], 0x48\n"
      "s_load_dwordx2 s[12:13], s[0:1], 0x58\n"
      "v_lshrrev_b32 v111, 6, v0\n"
      "v_and_b32 v106, 63, v0\n"
      "v_readfirstlane_b32 s33, v111\n"
      "v_lshrrev_b32 v74, 4, v106\n"
      "v_and_b32 v73, 15, v106\n"
      "v_lshlrev_b32 v108, 4, v106\n"
      "s_getpc_b64 s[24:25]\n"
      "L_pc:\n"
      "s_sub_u32 s26, s24, s20\n"
      "s_bfe_u32 s43, s2, 0x30003\n"
      "s_lshl3_add_u32 s43, s43, s33\n"
      "s_mul_i32 s43, s43, 0x200\n"
      "v_lshlrev_b32 v110, 7, v106\n"
      "v_add_u32 v110, s43, v110\n"
      "v_min_u32 v110, L_codeend-L_pc-4, v110\n"
      "v_add_u32 v110, s26, v110\n"
      "s_lshl_b32 s43, s2, 6\n"
      "s_lshl_b32 s27, s33, 7\n"
      "s_add_u32 s27, s27, 0x4000\n"
      "v_writelane_b32 v110, s43, 4\n"
      "v_writelane_b32 v110, s27, 5\n"
      "s_mov_b32 s43, 48\n"
      "s_cmp_gt_i32 s26, 0\n"
      "s_cselect_b32 exec_lo, 63, s43\n"
      "s_mov_b32 exec_hi, 0\n"
      "global_load_dword v176, v110, s[20:21]\n"
      "s_mov_b64 exec, -1\n"
      "s_lshl3_add_u32 s43, s33, 8\n"
      "s_cmp_lt_u32 s33, 6\n"
      "s_cselect_b32 s43, s43, 0x50\n"
      "s_load_dwordx2 s[14:15], s[0:1], s43\n"
      "s_lshl_b32 s28, s33, 7\n"
      "s_add_u32 s28, s28, 0x4000\n"
      "s_and_b32 s34, s2, 7\n"
      "s_lshr_b32 s35, s2, 3\n"
      "v_and_b32 v111, 0x1f0, v108\n"
      "v_lshrrev_b32 v110, 5, v106\n"
      "v_lshl_add_u32 v111, v110, 10, v111\n"
      "s_and_b32 s43, s35, 1\n"
      "s_lshl_b32 s43, s43, 9\n"
      "v_add_u32 v254, s43, v111\n"
      "v_xor_b32 v255, 0x200, v254\n"
      "s_lshr_b32 s27, s35, 1\n"
      "s_lshl4_add_u32 s27, s34, s27\n"
      "s_lshl3_add_u32 s27, s27, s33\n"
      "s_lshl_b32 s27, s27, 11\n"
      "s_lshl_b32 s29, s34, 5\n"
      "s_add_u32 s29, s29, s35\n"
      "s_lshl_b32 s29, s29, 2\n"
      "s_lshr_b32 s36, s33, 1\n"
      "s_and_b32 s37, s33, 1\n"
      "s_lshr_b32 s42, s33, 2\n"
      "s_mov_b32 s41, 0x40000\n"
      "s_lshl_b32 s43, s33, 9\n"
      "s_add_u32 s43, s43, 0x271a0\n"
      "s_lshl_b32 s26, s29, 7\n"
      "s_and_b32 s38, s34, 1\n"
      "s_and_b32 s39, s35, 7\n"
      "s_lshl3_add_u32 s38, s38, s39\n"
      "s_mul_i32 s38, s38, 0xe0\n"
      "s_cmp_lt_u32 s33, 6\n"
      "s_cselect_b32 m0, s43, 0x27dc0\n"
      "s_cselect_b32 s26, s26, s38\n"
      "s_cselect_b32 s38, -1, 0x3fff\n"
      "s_waitcnt lgkmcnt(0)\n"
      "s_add_u32 s24, s4, s27\n"
      "s_addc_u32 s25, s5, 0\n"
      "global_load_dwordx4 v[2:5], v254, s[24:25]\n"
      "global_load_dwordx4 v[6:9], v255, s[24:25]\n"
      "s_add_u32 s14, s14, s26\n"
      "s_addc_u32 s15, s15, 0\n"
      "s_mov_b32 exec_hi, 0\n"
      "s_mov_b32 exec_lo, s38\n"
      "global_load_lds_dwordx4 v108, s[14:15]\n"
      "s_mov_b64 exec, -1\n"
      "s_lshl_b32 s43, s2, 6\n"
      "s_load_dwordx16 s[44:59], s[20:21], s43\n"
      "s_load_dwordx16 s[60:75], s[20:21], s28\n"
      "s_load_dword s38, s[18:19], 0\n"
      "s_load_dword s39, s[8:9], s29\n"
      "s_load_dword s40, s[10:11], s29\n"
      "s_mov_b32 s22, 0xd20\n"
      "s_mov_b32 s23, 0xd1e2f30\n"
      "s_mov_b32 s27, 0xe00\n"
      "v_lshlrev_b32 v109, 4, v0\n"
      "v_add_u32 v111, 0, v0\n"
      "v_mul_u32_u24 v111, 0x124a, v111\n"
      "v_lshrrev_b32 v111, 16, v111\n"
      "v_add_u32 v110, 0, v109\n"
      "v_mad_u32_u24 v110, v111, s22, v110\n"
      "v_and_b32 v111, 7, v111\n"
      "v_lshlrev_b32 v111, 2, v111\n"
      "v_lshrrev_b32 v111, v111, s23\n"
      "v_bfe_i32 v111, v111, 0, 4\n"
      "v_mad_i32_i24 v75, v111, s27, v110\n"
      "v_add_u32 v111, 0x200, v0\n"
      "v_mul_u32_u24 v111, 0x124a, v111\n"
      "v_lshrrev_b32 v111, 16, v111\n"
      "v_add_u32 v110, 0x2000, v109\n"
      "v_mad_u32_u24 v110, v111, s22, v110\n"
      "v_and_b32 v111, 7, v111\n"
      "v_lshlrev_b32 v111, 2, v111\n"
      "v_lshrrev_b32 v111, v111, s23\n"
      "v_bfe_i32 v111, v111, 0, 4\n"
      "v_mad_i32_i24 v76, v111, s27, v110\n"
      "v_add_u32 v111, 0x400, v0\n"
      "v_mul_u32_u24 v111, 0x124a, v111\n"
      "v_lshrrev_b32 v111, 16, v111\n"
      "v_add_u32 v110, 0x4000, v109\n"
      "v_mad_u32_u24 v110, v111, s22, v110\n"
      "v_and_b32 v111, 7, v111\n"
      "v_lshlrev_b32 v111, 2, v111\n"
      "v_lshrrev_b32 v111, v111, s23\n"
      "v_bfe_i32 v111, v111, 0, 4\n"
      "v_mad_i32_i24 v77, v111, s27, v110\n"
      "v_add_u32 v111, 0x600, v0\n"
      "v_mul_u32_u24 v111, 0x124a, v111\n"
      "v_lshrrev_b32 v111, 16, v111\n"
      "v_add_u32 v110, 0x6000, v109\n"
      "v_mad_u32_u24 v110, v111, s22, v110\n"
      "v_and_b32 v111, 7, v111\n"
      "v_lshlrev_b32 v111, 2, v111\n"
      "v_lshrrev_b32 v111, v111, s23\n"
      "v_bfe_i32 v111, v111, 0, 4\n"
      "v_mad_i32_i24 v78, v111, s27, v110\n"
      "v_add_u32 v111, 0x800, v0\n"
      "v_mul_u32_u24 v111, 0x124a, v111\n"
      "v_lshrrev_b32 v111, 16, v111\n"
      "v_add_u32 v110, 0x8000, v109\n"
      "v_mad_u32_u24 v110, v111, s22, v110\n"
      "v_and_b32 v111, 7, v111\n"
      "v_lshlrev_b32 v111, 2, v111\n"
      "v_lshrrev_b32 v111, v111, s23\n"
      "v_bfe_i32 v111, v111, 0, 4\n"
      "v_mad_i32_i24 v79, v111, s27, v110\n"
      "v_add_u32 v111, 0xa00, v0\n"
      "v_mul_u32_u24 v111, 0x124a, v111\n"
      "v_lshrrev_b32 v111, 16, v111\n"
      "v_add_u32 v110, 0xa000, v109\n"
      "v_mad_u32_u24 v110, v111, s22, v110\n"
      "v_and_b32 v111, 7, v111\n"
      "v_lshlrev_b32 v111, 2, v111\n"
      "v_lshrrev_b32 v111, v111, s23\n"
      "v_bfe_i32 v111, v111, 0, 4\n"
      "v_mad_i32_i24 v80, v111, s27, v110\n"
      "v_add_u32 v111, 0xc00, v0\n"
      "v_mul_u32_u24 v111, 0x124a, v111\n"
      "v_lshrrev_b32 v111, 16, v111\n"
      "v_add_u32 v110, 0xc000, v109\n"
      "v_mad_u32_u24 v110, v111, s22, v110\n"
      "v_and_b32 v111, 7, v111\n"
      "v_lshlrev_b32 v111, 2, v111\n"
      "v_lshrrev_b32 v111, v111, s23\n"
      "v_bfe_i32 v111, v111, 0, 4\n"
      "v_mad_i32_i24 v81, v111, s27, v110\n"
      "s_sub_u32 s4, s33, 6\n"
      "s_lshl_b32 s4, s4, 5\n"
      "s_add_u32 s4, s4, s35\n"
      "s_cmp_lt_u32 s4, 56\n"
      "s_cbranch_scc0 L_skip_1\n"
      "v_lshrrev_b32 v111, 1, v74\n"
      "v_lshlrev_b32 v111, 4, v111\n"
      "v_and_b32 v110, 1, v74\n"
      "v_lshl_add_u32 v111, v110, 2, v111\n"
      "v_and_b32 v110, 8, v106\n"
      "v_add_u32 v111, v111, v110\n"
      "v_and_b32 v110, 0x70, v108\n"
      "v_lshl_add_u32 v111, v111, 10, v110\n"
      "s_lshl_b32 s43, s37, 14\n"
      "s_waitcnt lgkmcnt(0)\n"
      "s_add_u32 s43, s43, s46\n"
      "s_add_u32 s8, s18, s43\n"
      "s_addc_u32 s9, s19, 0\n"
      "s_lshl_b32 s43, s37, 17\n"
      "s_add_u32 s43, s43, s45\n"
      "s_add_u32 s12, s12, s43\n"
      "s_addc_u32 s13, s13, 0\n"
      "global_load_dwordx4 v[112:115], v111, s[12:13] offset:0\n"
      "global_load_dwordx4 v[116:119], v111, s[12:13] offset:1024\n"
      "global_load_dwordx4 v[120:123], v111, s[12:13] offset:2048\n"
      "global_load_dwordx4 v[124:127], v111, s[12:13] offset:3072\n"
      "L_skip_1:\n"
      "v_mov_b32 v34, -1\n"
      "v_mov_b32 v35, -1\n"
      "v_mov_b32 v36, -1\n"
      "v_mov_b32 v37, -1\n"
      "s_waitcnt lgkmcnt(0)\n"
      "s_add_u32 s10, s18, s44\n"
      "s_addc_u32 s11, s19, 0\n"
      "v_lshlrev_b32 v111, 4, v0\n"
      "global_store_dwordx4 v111, v[34:37], s[10:11] sc1\n"
      "s_lshl_b32 s43, s37, 4\n"
      "v_and_b32 v111, 31, v106\n"
      "v_xor_b32 v104, s43, v111\n"
      "s_add_u32 s6, s6, s58\n"
      "s_addc_u32 s7, s7, 0\n"
      "s_add_u32 s43, s47, s73\n"
      "s_add_u32 s12, s18, s43\n"
      "s_addc_u32 s13, s19, 0\n"
      "s_lshl_b32 s5, s33, 10\n"
      "s_mul_i32 s43, s33, 0x420\n"
      "s_add_u32 s43, s43, 0xe000\n"
      "v_lshrrev_b32 v111, 5, v106\n"
      "v_mul_u32_u24 v110, 16, v111\n"
      "v_add3_u32 v72, v110, v108, s43\n"
      "v_lshl_add_u32 v69, v111, 2, s60\n"
      "v_and_b32 v111, 1, v106\n"
      "v_bfe_u32 v110, v106, 2, 2\n"
      "v_lshl_add_u32 v111, v111, 3, v110\n"
      "v_mul_u32_u24 v111, 0x210, v111\n"
      "v_bfe_u32 v110, v106, 1, 1\n"
      "v_lshl_add_u32 v110, v74, 1, v110\n"
      "v_lshl_add_u32 v111, v110, 3, v111\n"
      "v_add_u32 v71, s61, v111\n"
      "v_lshrrev_b32 v111, 3, v106\n"
      "v_and_b32 v110, 0x70, v108\n"
      "v_lshl_add_u32 v111, v111, 14, v110\n"
      "v_add_u32 v70, s74, v111\n"
      "s_add_u32 s24, s18, s48\n"
      "s_addc_u32 s25, s19, 0\n"
      "s_add_u32 s10, s18, s49\n"
      "s_addc_u32 s11, s19, 0\n"
      "s_mov_b32 s14, 0x3f803f80\n"
      "s_mov_b32 s15, 0x3b000000\n"
      "s_mov_b32 s29, 0xfffcfffc\n"
      "v_mov_b32 v38, 0\n"
      "v_mov_b32 v39, 0\n"
      "v_mov_b32 v40, 0\n"
      "v_mov_b32 v41, 0\n"
      "v_lshrrev_b32 v111, 5, v106\n"
      "v_mul_u32_u24 v111, 0x1040, v111\n"
      "v_add_u32 v111, v111, v108\n"
      "v_add_u32 v111, 0x16200, v111\n"
      "ds_write_b128 v111, v[38:41]\n"
      "v_and_b32 v111, 1, v106\n"
      "v_mul_u32_u24 v111, 0xc10, v111\n"
      "v_add_u32 v111, 0xcc08, v111\n"
      "ds_write_b32 v111, v38\n"
      "v_mov_b32 v246, 0\n"
      "v_mov_b32 v247, 0\n"
      "v_mov_b32 v248, 0\n"
      "v_mov_b32 v249, 0\n"
      "v_mov_b32 v250, 0\n"
      "v_mov_b32 v251, 0\n"
      "v_mov_b32 v252, 0\n"
      "v_mov_b32 v253, 0\n"
      "v_mov_b32 v254, 0\n"
      "v_mov_b32 v255, 0\n"
      "v_and_b32 v111, 0x7f, v0\n"
      "v_lshl_add_u32 v67, v111, 1, s62\n"
      "v_lshl_add_u32 v66, v111, 2, s63\n"
      "v_mul_u32_u24 v111, 0x210, v73\n"
      "v_lshl_add_u32 v111, v74, 4, v111\n"
      "v_add_u32 v64, s64, v111\n"
      "v_add_u32 v65, s65, v64\n"
      "s_cmp_eq_u32 s67, 0\n"
      "s_cbranch_scc0 L_skip_2\n"
      "v_lshrrev_b32 v111, 1, v74\n"
      "v_lshlrev_b32 v111, 7, v111\n"
      "v_and_b32 v110, 1, v74\n"
      "v_lshl_add_u32 v111, v110, 2, v111\n"
      "v_lshl_add_u32 v111, v73, 3, v111\n"
      "v_cvt_f32_u32 v111, v111\n"
      "v_add_f32 v110, 0, v111\n"
      "v_cvt_pk_bf16_f32 v110, v110, 0\n"
      "v_max_u32 v110, 0x3f7f, v110\n"
      "v_lshl_add_u32 v29, v110, 1, s66\n"
      "v_add_f32 v110, 1.0, v111\n"
      "v_cvt_pk_bf16_f32 v110, v110, 0\n"
      "v_lshl_add_u32 v30, v110, 1, s66\n"
      "v_add_f32 v110, 2.0, v111\n"
      "v_cvt_pk_bf16_f32 v110, v110, 0\n"
      "v_lshl_add_u32 v31, v110, 1, s66\n"
      "v_add_f32 v110, 0x40400000, v111\n"
      "v_cvt_pk_bf16_f32 v110, v110, 0\n"
      "v_lshl_add_u32 v32, v110, 1, s66\n"
      "s_branch L_end_3\n"
      "L_skip_2:\n"
      "s_cmp_eq_u32 s67, 1\n"
      "s_cbranch_scc0 L_skip_4\n"
      "v_cmp_gt_u32 s[22:23], 2, v74\n"
      "v_lshlrev_b32 v111, 1, v73\n"
      "v_lshlrev_b32 v110, 2, v73\n"
      "v_cndmask_b32 v111, v111, v110, s[22:23]\n"
      "v_lshl_add_u32 v111, v74, 6, v111\n"
      "v_lshl_add_u32 v29, v111, 1, s66\n"
      "v_add_u32 v30, 2, v29\n"
      "v_mov_b32 v111, 64\n"
      "v_mov_b32 v110, 4\n"
      "v_cndmask_b32 v111, v111, v110, s[22:23]\n"
      "v_add_u32 v31, v111, v29\n"
      "v_add_u32 v32, 2, v31\n"
      "s_branch L_end_5\n"
      "L_skip_4:\n"
      "v_lshl_add_u32 v111, v74, 6, v73\n"
      "v_lshl_add_u32 v29, v111, 1, s66\n"
      "v_add_u32 v30, 32, v29\n"
      "v_add_u32 v31, 64, v29\n"
      "v_add_u32 v32, 0x60, v29\n"
      "L_end_5:\n"
      "L_end_3:\n"
      "s_add_u32 s30, s18, s50\n"
      "s_addc_u32 s31, s19, 0\n"
      "v_lshrrev_b32 v111, 1, v0\n"
      "v_and_b32 v110, 1, v0\n"
      "v_lshlrev_b32 v109, 6, v111\n"
      "v_lshl_add_u32 v63, v110, 4, v109\n"
      "v_mul_u32_u24 v110, 0x1240, v110\n"
      "v_lshl_add_u32 v110, v111, 4, v110\n"
      "v_add_u32 v62, 0x16400, v110\n"
      "s_add_u32 s43, s51, s5\n"
      "s_add_u32 s46, s18, s43\n"
      "s_addc_u32 s47, s19, 0\n"
      "s_add_u32 s44, s5, 0x18680\n"
      "s_lshl_b32 s43, s38, 1\n"
      "s_xor_b32 s43, s43, s38\n"
      "s_and_b32 s43, s43, 0xaaaaaaaa\n"
      "s_andn2_b32 s38, 0x55555555, s38\n"
      "s_or_b32 s38, s38, s43\n"
      "v_lshlrev_b32 v111, 1, v106\n"
      "v_and_b32 v111, 8, v111\n"
      "v_lshrrev_b32 v111, v111, s38\n"
      "v_and_b32 v82, 0x30003, v111\n"
      "v_lshrrev_b32 v111, 2, v111\n"
      "v_and_b32 v83, 0x30003, v111\n"
      "v_lshrrev_b32 v111, 2, v111\n"
      "v_and_b32 v84, 0x30003, v111\n"
      "v_lshrrev_b32 v111, 2, v111\n"
      "v_and_b32 v85, 0x30003, v111\n"
      "v_lshlrev_b32 v111, 2, v106\n"
      "v_and_b32 v111, 8, v111\n"
      "v_lshrrev_b32 v111, v111, s38\n"
      "v_and_b32 v86, 0x30003, v111\n"
      "v_lshrrev_b32 v111, 2, v111\n"
      "v_and_b32 v87, 0x30003, v111\n"
      "v_lshrrev_b32 v111, 2, v111\n"
      "v_and_b32 v88, 0x30003, v111\n"
      "v_lshrrev_b32 v111, 2, v111\n"
      "v_and_b32 v89, 0x30003, v111\n"
      "s_lshl_b32 s43, s39, 16\n"
      "s_and_b32 s26, s39, 0xffff0000\n"
      "s_cmp_eq_u32 s42, 0\n"
      "s_cselect_b32 s43, s43, s26\n"
      "v_mov_b32 v109, 0x3fb8aa3b\n"
      "v_mul_f32 v111, s43, v109\n"
      "v_exp_f32 v111, v111\n"
      "s_nop 0\n"
      "v_cvt_pk_bf16_f32 v61, 0, v111\n"
      "v_lshlrev_b32 v111, 2, v106\n"
      "v_and_b32 v111, 16, v111\n"
      "v_lshrrev_b32 v111, v111, s40\n"
      "v_lshlrev_b32 v90, 16, v111\n"
      "v_and_b32 v111, 0xff, v0\n"
      "v_lshl_add_u32 v68, v111, 4, s68\n"
      "v_cvt_f32_u32 v111, v111\n"
      "v_mov_b32 v160, 0x44ffe000\n"
      "v_fmamk_f32 v161, v111, 0xc1000000, v160\n"
      "v_mov_b32 v160, 0x3f7f\n"
      "v_mov_b32 v163, 0x4583\n"
      "v_subrev_f32 v111, 0, v161\n"
      "v_subrev_f32 v110, 1.0, v161\n"
      "v_cvt_pk_bf16_f32 v111, v111, v110\n"
      "v_and_b32 v110, 0xffff, v111\n"
      "v_lshl_add_u32 v18, v110, 1, s69\n"
      "v_lshrrev_b32 v111, 16, v111\n"
      "v_lshl_add_u32 v19, v111, 1, s69\n"
      "v_subrev_f32 v111, 2.0, v161\n"
      "v_subrev_f32 v110, 0x40400000, v161\n"
      "v_cvt_pk_bf16_f32 v111, v111, v110\n"
      "v_and_b32 v110, 0xffff, v111\n"
      "v_lshl_add_u32 v20, v110, 1, s69\n"
      "v_lshrrev_b32 v111, 16, v111\n"
      "v_lshl_add_u32 v21, v111, 1, s69\n"
      "v_subrev_f32 v111, 4.0, v161\n"
      "v_subrev_f32 v110, 0x40a00000, v161\n"
      "v_cvt_pk_bf16_f32 v111, v111, v110\n"
      "v_and_b32 v110, 0xffff, v111\n"
      "v_lshl_add_u32 v22, v110, 1, s69\n"
      "v_lshrrev_b32 v111, 16, v111\n"
      "v_lshl_add_u32 v23, v111, 1, s69\n"
      "v_subrev_f32 v111, 0x40c00000, v161\n"
      "v_subrev_f32 v110, 0x40e00000, v161\n"
      "v_cvt_pk_bf16_f32 v111, v111, v110\n"
      "v_and_b32 v110, 0xffff, v111\n"
      "v_med3_u32 v110, v110, v160, v163\n"
      "v_lshl_add_u32 v24, v110, 1, s69\n"
      "v_lshrrev_b32 v111, 16, v111\n"
      "v_med3_u32 v111, v111, v160, v163\n"
      "v_lshl_add_u32 v25, v111, 1, s69\n"
      "v_subrev_f32 v111, 0x41000000, v161\n"
      "v_subrev_f32 v110, 0x41100000, v161\n"
      "v_cvt_pk_bf16_f32 v111, v111, v110\n"
      "v_and_b32 v110, 0xffff, v111\n"
      "v_med3_u32 v110, v110, v160, v163\n"
      "v_lshl_add_u32 v26, v110, 1, s69\n"
      "v_lshrrev_b32 v111, 16, v111\n"
      "v_med3_u32 v111, v111, v160, v163\n"
      "v_lshl_add_u32 v27, v111, 1, s69\n"
      "v_subrev_f32 v111, 0x41200000, v161\n"
      "v_subrev_f32 v110, 0x41300000, v161\n"
      "v_cvt_pk_bf16_f32 v111, v111, v110\n"
      "v_and_b32 v110, 0xffff, v111\n"
      "v_med3_u32 v110, v110, v160, v163\n"
      "v_lshl_add_u32 v28, v110, 1, s69\n"
      "v_and_b32 v111, 3, v73\n"
      "v_mul_u32_u24 v111, 0x1040, v111\n"
      "v_and_b32 v110, 12, v73\n"
      "v_lshlrev_b32 v110, 1, v110\n"
      "v_sub_u32 v111, v111, v110\n"
      "v_lshlrev_b32 v110, 4, v74\n"
      "v_add3_u32 v95, v111, v110, s70\n"
      "v_lshl_add_u32 v111, v73, 5, v110\n"
      "v_add_u32 v94, s71, v111\n"
      "v_mul_u32_u24 v111, 0x50, v73\n"
      "v_add3_u32 v93, v111, v110, s72\n"
      "v_lshrrev_b32 v111, 3, v106\n"
      "v_and_b32 v110, 0x70, v108\n"
      "v_lshl_add_u32 v110, v111, 15, v110\n"
      "v_add_u32 v97, s75, v110\n"
      "v_lshlrev_b32 v111, 5, v111\n"
      "v_and_b32 v110, 3, v106\n"
      "v_lshl_add_u32 v111, v110, 3, v111\n"
      "v_and_b32 v110, 0x7f, v111\n"
      "v_lshl_add_u32 v91, v110, 3, s44\n"
      "v_lshrrev_b32 v110, 5, v106\n"
      "v_lshl_add_u32 v91, v110, 13, v91\n"
      "s_lshl_b32 s43, s33, 8\n"
      "v_add_u32 v111, s43, v111\n"
      "v_bfe_u32 v109, v106, 2, 1\n"
      "v_mul_u32_u24 v110, 0x1240, v109\n"
      "v_lshl_add_u32 v110, v111, 1, v110\n"
      "v_add_u32 v92, 0x16400, v110\n"
      "v_lshrrev_b32 v110, 4, v111\n"
      "v_lshl_add_u32 v111, v110, 2, v111\n"
      "v_mul_u32_u24 v110, 0x2800, v109\n"
      "v_lshl_add_u32 v111, v111, 2, v110\n"
      "v_add_u32 v98, 0xe000, v111\n"
      "s_add_u32 s28, s28, 64\n"
      "s_load_dwordx16 s[60:75], s[20:21], s28\n"
      "s_add_u32 s20, s46, 0\n"
      "s_addc_u32 s21, s47, 0\n"
      "s_cmp_eq_u32 s33, 7\n"
      "s_cbranch_scc1 L_skip_6\n"
      "global_store_dwordx4 v108, v[34:37], s[20:21] sc1\n"
      "L_skip_6:\n"
      "s_add_u32 s20, s46, 0x1c000\n"
      "s_addc_u32 s21, s47, 0\n"
      "s_cmp_eq_u32 s33, 7\n"
      "s_cbranch_scc1 L_skip_7\n"
      "global_store_dwordx4 v108, v[34:37], s[20:21] sc1\n"
      "L_skip_7:\n"
      "s_cmp_lt_u32 s4, 56\n"
      "s_cbranch_scc1 L_w_8\n"
      "s_waitcnt vmcnt(2)\n"
      "L_w_8:\n"
      "s_waitcnt vmcnt(6)\n"
      "v_dot2_f32_bf16 v242, v2, s14, 0\n"
      "v_dot2_f32_bf16 v244, v2, v2, 0\n"
      "v_dot2_f32_bf16 v243, v3, s14, 0\n"
      "v_dot2_f32_bf16 v245, v3, v3, 0\n"
      "v_dot2_f32_bf16 v242, v4, s14, v242\n"
      "v_dot2_f32_bf16 v244, v4, v4, v244\n"
      "v_dot2_f32_bf16 v243, v5, s14, v243\n"
      "v_dot2_f32_bf16 v245, v5, v5, v245\n"
      "v_dot2_f32_bf16 v242, v6, s14, v242\n"
      "v_dot2_f32_bf16 v244, v6, v6, v244\n"
      "v_dot2_f32_bf16 v243, v7, s14, v243\n"
      "v_dot2_f32_bf16 v245, v7, v7, v245\n"
      "v_dot2_f32_bf16 v242, v8, s14, v242\n"
      "v_dot2_f32_bf16 v244, v8, v8, v244\n"
      "v_dot2_f32_bf16 v243, v9, s14, v243\n"
      "v_dot2_f32_bf16 v245, v9, v9, v245\n"
      "s_add_u32 m0, s5, 0\n"
      "s_nop 0\n"
      "global_load_lds_dwordx4 v75, s[6:7]\n"
      "s_add_u32 m0, s5, 0x2000\n"
      "s_nop 0\n"
      "global_load_lds_dwordx4 v76, s[6:7]\n"
      "v_lshlrev_b32 v234, 16, v2\n"
      "v_and_b32 v235, 0xffff0000, v2\n"
      "v_add_f32 v242, v242, v243\n"
      "v_add_f32 v244, v244, v245\n"
      "v_lshlrev_b32 v236, 16, v3\n"
      "v_and_b32 v237, 0xffff0000, v3\n"
      "v_permlane16_swap_b32 v242, v244\n"
      "v_add_f32 v242, v242, v244\n"
      "v_lshlrev_b32 v238, 16, v4\n"
      "s_nop 0\n"
      "v_add_f32_dpp v242, v242, v242 row_ror:8 row_mask:0xf bank_mask:0xf\n"
      "v_and_b32 v239, 0xffff0000, v4\n"
      "s_nop 0\n"
      "v_add_f32_dpp v242, v242, v242 row_ror:4 row_mask:0xf bank_mask:0xf\n"
      "v_lshlrev_b32 v240, 16, v5\n"
      "s_nop 0\n"
      "v_add_f32_dpp v242, v242, v242 row_ror:2 row_mask:0xf bank_mask:0xf\n"
      "v_and_b32 v241, 0xffff0000, v5\n"
      "s_nop 0\n"
      "v_add_f32_dpp v242, v242, v242 row_ror:1 row_mask:0xf bank_mask:0xf\n"
      "v_mov_b32 v244, v242\n"
      "s_nop 1\n"
      "v_permlane16_swap_b32 v242, v244\n"
      "v_mul_f32 v242, 0x3b000000, v242\n"
      "v_mul_f32 v244, 0x3b000000, v244\n"
      "v_fma_f32 v111, -v242, v242, v244\n"
      "v_max_f32 v111, 0, v111\n"
      "v_cvt_pk_bf16_f32 v111, 0, v111\n"
      "v_add_f32 v111, 0x3727c5ac, v111\n"
      "v_cvt_pk_bf16_f32 v111, 0, v111\n"
      "v_sqrt_f32 v111, v111\n"
      "s_nop 0\n"
      "v_cvt_pk_bf16_f32 v243, v242, v111\n"
      "v_and_b32 v111, 0xffff0000, v243\n"
      "v_rcp_f32 v111, v111\n"
      "v_lshlrev_b32 v110, 16, v243\n"
      "ds_write_b32 v69, v243\n"
      "v_mul_f32 v110, v110, v111\n"
      "v_fma_f32 v234, v234, v111, -v110\n"
      "v_fma_f32 v235, v235, v111, -v110\n"
      "v_fma_f32 v236, v236, v111, -v110\n"
      "v_fma_f32 v237, v237, v111, -v110\n"
      "v_fma_f32 v238, v238, v111, -v110\n"
      "v_fma_f32 v239, v239, v111, -v110\n"
      "v_fma_f32 v240, v240, v111, -v110\n"
      "v_fma_f32 v241, v241, v111, -v110\n"
      "v_cvt_pk_f16_f32 v2, v234, v235\n"
      "v_cvt_pk_f16_f32 v3, v236, v237\n"
      "v_cvt_pk_f16_f32 v4, v238, v239\n"
      "v_cvt_pk_f16_f32 v5, v240, v241\n"
      "ds_write_b128 v72, v[2:5]\n"
      "v_xor_b32 v109, 0x80, v67\n"
      "s_cmp_lt_u32 s4, 56\n"
      "s_cbranch_scc1 L_w_9\n"
      "s_waitcnt vmcnt(3)\n"
      "L_w_9:\n"
      "s_waitcnt vmcnt(7)\n"
      "s_waitcnt lgkmcnt(0)\n"
      "s_barrier\n"
      "ds_read_b64_tr_b16 v[2:3], v71\n"
      "ds_read_b64_tr_b16 v[4:5], v71 offset:2112\n"
      "ds_read_u16_d16_hi v248, v67 offset:30112\n"
      "ds_read_u16_d16_hi v249, v67 offset:30624\n"
      "ds_read_u16_d16_hi v250, v67 offset:31136\n"
      "ds_read_u16_d16_hi v251, v67 offset:31648\n"
      "ds_read_u16_d16_hi v252, v109 offset:30112\n"
      "ds_read_u16_d16_hi v253, v109 offset:30624\n"
      "ds_read_u16_d16_hi v254, v109 offset:31136\n"
      "ds_read_u16_d16_hi v255, v109 offset:31648\n"
      "ds_read_u16_d16_hi v246, v67 offset:29088\n"
      "ds_read_u16_d16_hi v247, v67 offset:29600\n"
      "s_waitcnt lgkmcnt(11)\n"
      "v_add_u32 v2, 0x20002, v2\n"
      "v_and_or_b32 v2, v2, s29, v82\n"
      "v_add_u32 v3, 0x20002, v3\n"
      "v_and_or_b32 v3, v3, s29, v83\n"
      "s_waitcnt lgkmcnt(10)\n"
      "v_add_u32 v4, 0x20002, v4\n"
      "v_and_or_b32 v4, v4, s29, v84\n"
      "v_add_u32 v5, 0x20002, v5\n"
      "v_and_or_b32 v5, v5, s29, v85\n"
      "global_store_dwordx4 v70, v[2:5], s[24:25] sc1\n"
      "s_waitcnt lgkmcnt(9)\n"
      "v_mul_f32 v248, v61, v248\n"
      "s_waitcnt lgkmcnt(8)\n"
      "v_mul_f32 v249, v61, v249\n"
      "s_waitcnt lgkmcnt(5)\n"
      "v_mul_f32 v252, v61, v252\n"
      "s_waitcnt lgkmcnt(4)\n"
      "v_mul_f32 v253, v61, v253\n"
      "v_mul_f32 v8, v250, v248\n"
      "v_mul_f32 v9, v250, v249\n"
      "s_waitcnt lgkmcnt(3)\n"
      "v_mul_f32 v111, v254, v252\n"
      "v_mul_f32 v110, v254, v253\n"
      "v_fma_f32 v8, -v251, v249, v8\n"
      "v_fmac_f32 v9, v251, v248\n"
      "s_waitcnt lgkmcnt(2)\n"
      "v_fma_f32 v111, -v255, v253, v111\n"
      "v_fmac_f32 v110, v255, v252\n"
      "v_add_f32 v109, |v8|, |v9|\n"
      "v_add_f32 v111, |v111|, |v110|\n"
      "v_add_f32 v111, v111, v109\n"
      "s_waitcnt lgkmcnt(1)\n"
      "v_mul_f32 v246, v61, v246\n"
      "s_waitcnt lgkmcnt(0)\n"
      "v_mul_f32 v247, v61, v247\n"
      "v_add_f32_dpp v111, v111, v111 row_ror:8 row_mask:0xf bank_mask:0xf\n"
      "v_mul_f32 v246, 0x3fb8aa3b, v246\n"
      "v_mul_f32 v247, 0x3e22f983, v247\n"
      "v_add_f32_dpp v111, v111, v111 row_ror:4 row_mask:0xf bank_mask:0xf\n"
      "v_mul_f32 v242, 0x43000000, v246\n"
      "v_mul_f32 v243, 0x43000000, v247\n"
      "v_add_f32_dpp v111, v111, v111 row_ror:2 row_mask:0xf bank_mask:0xf\n"
      "v_exp_f32 v242, v242\n"
      "v_fract_f32 v243, v243\n"
      "v_add_f32_dpp v111, v111, v111 row_ror:1 row_mask:0xf bank_mask:0xf\n"
      "v_cos_f32 v192, v243\n"
      "v_readlane_b32 s28, v111, 0\n"
      "v_readlane_b32 s45, v111, 16\n"
      "v_readlane_b32 s48, v111, 32\n"
      "v_readlane_b32 s49, v111, 48\n"
      "v_sin_f32 v193, v243\n"
      "v_mul_f32 v192, v192, v242\n"
      "v_mul_f32 v193, v193, v242\n"
      "s_max_u32 s28, s28, s45\n"
      "s_max_u32 s48, s48, s49\n"
      "s_max_u32 s28, s28, s48\n"
      "s_lshr_b32 s28, s28, 23\n"
      "s_max_u32 s28, s28, 16\n"
      "s_min_u32 s28, s28, 0xfa\n"
      "s_sub_u32 s45, 0x10a, s28\n"
      "s_lshl_b32 s45, s45, 23\n"
      "s_sub_u32 s48, s28, 12\n"
      "s_lshl_b32 s48, s48, 23\n"
      "v_mul_f32 v8, s45, v8\n"
      "v_mul_f32 v9, s45, v9\n"
      "s_lshl2_add_u32 s49, s42, 0x27db0\n"
      "v_mov_b32 v111, s49\n"
      "v_mov_b32 v110, s48\n"
      "ds_write_b32 v111, v110\n"
      "s_bitcmp0_b32 s33, 1\n"
      "s_cbranch_scc0 L_skip_10\n"
      "v_mul_f32 v242, 1.0, v246\n"
      "v_mul_f32 v244, 0x41000000, v246\n"
      "v_mul_f32 v243, 1.0, v247\n"
      "v_mul_f32 v245, 0x41000000, v247\n"
      "v_exp_f32 v242, v242\n"
      "v_exp_f32 v244, v244\n"
      "v_fract_f32 v243, v243\n"
      "v_fract_f32 v245, v245\n"
      "v_cos_f32 v194, v243\n"
      "v_cos_f32 v196, v245\n"
      "v_sin_f32 v195, v243\n"
      "v_sin_f32 v197, v245\n"
      "v_mul_f32 v194, v194, v242\n"
      "v_mul_f32 v196, v196, v244\n"
      "v_mul_f32 v195, v195, v242\n"
      "v_mul_f32 v197, v197, v244\n"
      "v_mul_f32 v242, 0x42800000, v246\n"
      "v_pk_mul_f32 v[206:207], v[8:9], v[192:193] op_sel:[1,1] op_sel_hi:[1,0]\n"
      "v_mul_f32 v243, 0x42800000, v247\n"
      "v_pk_fma_f32 v[206:207], v[8:9], v[192:193], v[206:207] op_sel:[0,0,0] op_sel_hi:[0,1,1] neg_lo:[0,0,1]\n"
      "v_exp_f32 v242, v242\n"
      "v_fract_f32 v243, v243\n"
      "v_cos_f32 v198, v243\n"
      "v_sin_f32 v199, v243\n"
      "v_mul_f32 v198, v198, v242\n"
      "v_mul_f32 v199, v199, v242\n"
      "v_mov_b32 v200, 1.0\n"
      "v_mov_b32 v201, 0\n"
      "v_cvt_pk_f16_f32 v238, v8, -v9\n"
      "ds_write_b32 v66, v238 offset:0\n"
      "v_pk_mul_f32 v[204:205], v[8:9], v[194:195] op_sel:[1,1] op_sel_hi:[1,0]\n"
      "v_cvt_pk_f16_f32 v239, v206, -v207\n"
      "ds_write_b32 v66, v239 offset:4224\n"
      "v_pk_fma_f32 v[204:205], v[8:9], v[194:195], v[204:205] op_sel:[0,0,0] op_sel_hi:[0,1,1] neg_lo:[0,0,1]\n"
      "v_pk_mul_f32 v[208:209], v[206:207], v[194:195] op_sel:[1,1] op_sel_hi:[1,0]\n"
      "v_cvt_pk_f16_f32 v240, v200, v201\n"
      "ds_write_b32 v66, v240 offset:21648\n"
      "v_pk_fma_f32 v[208:209], v[206:207], v[194:195], v[208:209] op_sel:[0,0,0] op_sel_hi:[0,1,1] neg_lo:[0,0,1]\n"
      "v_cvt_pk_f16_f32 v241, v198, v199\n"
      "ds_write_b32 v66, v241 offset:25872\n"
      "v_pk_mul_f32 v[216:217], v[198:199], v[196:197] op_sel:[1,1] op_sel_hi:[1,0]\n"
      "v_pk_fma_f32 v[216:217], v[198:199], v[196:197], v[216:217] op_sel:[0,0,0] op_sel_hi:[0,1,1] neg_lo:[0,0,1]\n"
      "s_add_u32 m0, s5, 0x4000\n"
      "s_nop 0\n"
      "global_load_lds_dwordx4 v77, s[6:7]\n"
      "v_cvt_pk_f16_f32 v238, v204, -v205\n"
      "ds_write_b32 v66, v238 offset:528\n"
      "v_pk_mul_f32 v[202:203], v[204:205], v[194:195] op_sel:[1,1] op_sel_hi:[1,0]\n"
      "v_cvt_pk_f16_f32 v239, v208, -v209\n"
      "ds_write_b32 v66, v239 offset:4752\n"
      "v_pk_fma_f32 v[202:203], v[204:205], v[194:195], v[202:203] op_sel:[0,0,0] op_sel_hi:[0,1,1] neg_lo:[0,0,1]\n"
      "v_pk_mul_f32 v[206:207], v[208:209], v[194:195] op_sel:[1,1] op_sel_hi:[1,0]\n"
      "v_cvt_pk_f16_f32 v240, v196, v197\n"
      "ds_write_b32 v66, v240 offset:22176\n"
      "v_pk_fma_f32 v[206:207], v[208:209], v[194:195], v[206:207] op_sel:[0,0,0] op_sel_hi:[0,1,1] neg_lo:[0,0,1]\n"
      "v_pk_mul_f32 v[210:211], v[196:197], v[196:197] op_sel:[1,1] op_sel_hi:[1,0]\n"
      "v_cvt_pk_f16_f32 v241, v216, v217\n"
      "ds_write_b32 v66, v241 offset:26400\n"
      "v_pk_fma_f32 v[210:211], v[196:197], v[196:197], v[210:211] op_sel:[0,0,0] op_sel_hi:[0,1,1] neg_lo:[0,0,1]\n"
      "v_pk_mul_f32 v[214:215], v[216:217], v[196:197] op_sel:[1,1] op_sel_hi:[1,0]\n"
      "v_pk_fma_f32 v[214:215], v[216:217], v[196:197], v[214:215] op_sel:[0,0,0] op_sel_hi:[0,1,1] neg_lo:[0,0,1]\n"
      "v_cvt_pk_f16_f32 v238, v202, -v203\n"
      "ds_write_b32 v66, v238 offset:1056\n"
      "v_pk_mul_f32 v[204:205], v[202:203], v[194:195] op_sel:[1,1] op_sel_hi:[1,0]\n"
      "v_cvt_pk_f16_f32 v239, v206, -v207\n"
      "ds_write_b32 v66, v239 offset:5280\n"
      "v_pk_fma_f32 v[204:205], v[202:203], v[194:195], v[204:205] op_sel:[0,0,0] op_sel_hi:[0,1,1] neg_lo:[0,0,1]\n"
      "v_pk_mul_f32 v[208:209], v[206:207], v[194:195] op_sel:[1,1] op_sel_hi:[1,0]\n"
      "v_cvt_pk_f16_f32 v240, v210, v211\n"
      "ds_write_b32 v66, v240 offset:22704\n"
      "v_pk_fma_f32 v[208:209], v[206:207], v[194:195], v[208:209] op_sel:[0,0,0] op_sel_hi:[0,1,1] neg_lo:[0,0,1]\n"
      "v_pk_mul_f32 v[212:213], v[210:211], v[196:197] op_sel:[1,1] op_sel_hi:[1,0]\n"
      "v_cvt_pk_f16_f32 v241, v214, v215\n"
      "ds_write_b32 v66, v241 offset:26928\n"
      "v_pk_fma_f32 v[212:213], v[210:211], v[196:197], v[212:213] op_sel:[0,0,0] op_sel_hi:[0,1,1] neg_lo:[0,0,1]\n"
      "v_pk_mul_f32 v[216:217], v[214:215], v[196:197] op_sel:[1,1] op_sel_hi:[1,0]\n"
      "v_pk_fma_f32 v[216:217], v[214:215], v[196:197], v[216:217] op_sel:[0,0,0] op_sel_hi:[0,1,1] neg_lo:[0,0,1]\n"
      "s_add_u32 m0, s5, 0x6000\n"
      "s_nop 0\n"
      "global_load_lds_dwordx4 v78, s[6:7]\n"
      "v_cvt_pk_f16_f32 v238, v204, -v205\n"
      "ds_write_b32 v66, v238 offset:1584\n"
      "v_pk_mul_f32 v[202:203], v[204:205], v[194:195] op_sel:[1,1] op_sel_hi:[1,0]\n"
      "v_cvt_pk_f16_f32 v239, v208, -v209\n"
      "ds_write_b32 v66, v239 offset:5808\n"
      "v_pk_fma_f32 v[202:203], v[204:205], v[194:195], v[202:203] op_sel:[0,0,0] op_sel_hi:[0,1,1] neg_lo:[0,0,1]\n"
      "v_pk_mul_f32 v[206:207], v[208:209], v[194:195] op_sel:[1,1] op_sel_hi:[1,0]\n"
      "v_cvt_pk_f16_f32 v240, v212, v213\n"
      "ds_write_b32 v66, v240 offset:23232\n"
      "v_pk_fma_f32 v[206:207], v[208:209], v[194:195], v[206:207] op_sel:[0,0,0] op_sel_hi:[0,1,1] neg_lo:[0,0,1]\n"
      "v_pk_mul_f32 v[210:211], v[212:213], v[196:197] op_sel:[1,1] op_sel_hi:[1,0]\n"
      "v_cvt_pk_f16_f32 v241, v216, v217\n"
      "ds_write_b32 v66, v241 offset:27456\n"
      "v_pk_fma_f32 v[210:211], v[212:213], v[196:197], v[210:211] op_sel:[0,0,0] op_sel_hi:[0,1,1] neg_lo:[0,0,1]\n"
      "v_pk_mul_f32 v[214:215], v[216:217], v[196:197] op_sel:[1,1] op_sel_hi:[1,0]\n"
      "v_pk_fma_f32 v[214:215], v[216:217], v[196:197], v[214:215] op_sel:[0,0,0] op_sel_hi:[0,1,1] neg_lo:[0,0,1]\n"
      "v_cvt_pk_f16_f32 v238, v202, -v203\n"
      "ds_write_b32 v66, v238 offset:2112\n"
      "v_pk_mul_f32 v[204:205], v[202:203], v[194:195] op_sel:[1,1] op_sel_hi:[1,0]\n"
      "v_cvt_pk_f16_f32 v239, v206, -v207\n"
      "ds_write_b32 v66, v239 offset:6336\n"
      "v_pk_fma_f32 v[204:205], v[202:203], v[194:195], v[204:205] op_sel:[0,0,0] op_sel_hi:[0,1,1] neg_lo:[0,0,1]\n"
      "v_pk_mul_f32 v[208:209], v[206:207], v[194:195] op_sel:[1,1] op_sel_hi:[1,0]\n"
      "v_cvt_pk_f16_f32 v240, v210, v211\n"
      "ds_write_b32 v66, v240 offset:23760\n"
      "v_pk_fma_f32 v[208:209], v[206:207], v[194:195], v[208:209] op_sel:[0,0,0] op_sel_hi:[0,1,1] neg_lo:[0,0,1]\n"
      "v_pk_mul_f32 v[212:213], v[210:211], v[196:197] op_sel:[1,1] op_sel_hi:[1,0]\n"
      "v_cvt_pk_f16_f32 v241, v214, v215\n"
      "ds_write_b32 v66, v241 offset:27984\n"
      "v_pk_fma_f32 v[212:213], v[210:211], v[196:197], v[212:213] op_sel:[0,0,0] op_sel_hi:[0,1,1] neg_lo:[0,0,1]\n"
      "v_pk_mul_f32 v[216:217], v[214:215], v[196:197] op_sel:[1,1] op_sel_hi:[1,0]\n"
      "v_pk_fma_f32 v[216:217], v[214:215], v[196:197], v[216:217] op_sel:[0,0,0] op_sel_hi:[0,1,1] neg_lo:[0,0,1]\n"
      "s_add_u32 m0, s5, 0x8000\n"
      "s_nop 0\n"
      "global_load_lds_dwordx4 v79, s[6:7]\n"
      "v_cvt_pk_f16_f32 v238, v204, -v205\n"
      "ds_write_b32 v66, v238 offset:2640\n"
      "v_pk_mul_f32 v[202:203], v[204:205], v[194:195] op_sel:[1,1] op_sel_hi:[1,0]\n"
      "v_cvt_pk_f16_f32 v239, v208, -v209\n"
      "ds_write_b32 v66, v239 offset:6864\n"
      "v_pk_fma_f32 v[202:203], v[204:205], v[194:195], v[202:203] op_sel:[0,0,0] op_sel_hi:[0,1,1] neg_lo:[0,0,1]\n"
      "v_pk_mul_f32 v[206:207], v[208:209], v[194:195] op_sel:[1,1] op_sel_hi:[1,0]\n"
      "v_cvt_pk_f16_f32 v240, v212, v213\n"
      "ds_write_b32 v66, v240 offset:24288\n"
      "v_pk_fma_f32 v[206:207], v[208:209], v[194:195], v[206:207] op_sel:[0,0,0] op_sel_hi:[0,1,1] neg_lo:[0,0,1]\n"
      "v_pk_mul_f32 v[210:211], v[212:213], v[196:197] op_sel:[1,1] op_sel_hi:[1,0]\n"
      "v_cvt_pk_f16_f32 v241, v216, v217\n"
      "ds_write_b32 v66, v241 offset:28512\n"
      "v_pk_fma_f32 v[210:211], v[212:213], v[196:197], v[210:211] op_sel:[0,0,0] op_sel_hi:[0,1,1] neg_lo:[0,0,1]\n"
      "v_pk_mul_f32 v[214:215], v[216:217], v[196:197] op_sel:[1,1] op_sel_hi:[1,0]\n"
      "v_pk_fma_f32 v[214:215], v[216:217], v[196:197], v[214:215] op_sel:[0,0,0] op_sel_hi:[0,1,1] neg_lo:[0,0,1]\n"
      "v_cvt_pk_f16_f32 v238, v202, -v203\n"
      "ds_write_b32 v66, v238 offset:3168\n"
      "v_pk_mul_f32 v[204:205], v[202:203], v[194:195] op_sel:[1,1] op_sel_hi:[1,0]\n"
      "v_cvt_pk_f16_f32 v239, v206, -v207\n"
      "ds_write_b32 v66, v239 offset:7392\n"
      "v_pk_fma_f32 v[204:205], v[202:203], v[194:195], v[204:205] op_sel:[0,0,0] op_sel_hi:[0,1,1] neg_lo:[0,0,1]\n"
      "v_pk_mul_f32 v[208:209], v[206:207], v[194:195] op_sel:[1,1] op_sel_hi:[1,0]\n"
      "v_cvt_pk_f16_f32 v240, v210, v211\n"
      "ds_write_b32 v66, v240 offset:24816\n"
      "v_pk_fma_f32 v[208:209], v[206:207], v[194:195], v[208:209] op_sel:[0,0,0] op_sel_hi:[0,1,1] neg_lo:[0,0,1]\n"
      "v_pk_mul_f32 v[212:213], v[210:211], v[196:197] op_sel:[1,1] op_sel_hi:[1,0]\n"
      "v_cvt_pk_f16_f32 v241, v214, v215\n"
      "ds_write_b32 v66, v241 offset:29040\n"
      "v_pk_fma_f32 v[212:213], v[210:211], v[196:197], v[212:213] op_sel:[0,0,0] op_sel_hi:[0,1,1] neg_lo:[0,0,1]\n"
      "v_pk_mul_f32 v[216:217], v[214:215], v[196:197] op_sel:[1,1] op_sel_hi:[1,0]\n"
      "v_pk_fma_f32 v[216:217], v[214:215], v[196:197], v[216:217] op_sel:[0,0,0] op_sel_hi:[0,1,1] neg_lo:[0,0,1]\n"
      "s_add_u32 m0, s5, 0xa000\n"
      "s_nop 0\n"
      "global_load_lds_dwordx4 v80, s[6:7]\n"
      "v_cvt_pk_f16_f32 v238, v204, -v205\n"
      "ds_write_b32 v66, v238 offset:3696\n"
      "v_cvt_pk_f16_f32 v239, v208, -v209\n"
      "ds_write_b32 v66, v239 offset:7920\n"
      "v_cvt_pk_f16_f32 v240, v212, v213\n"
      "ds_write_b32 v66, v240 offset:25344\n"
      "v_cvt_pk_f16_f32 v241, v216, v217\n"
      "ds_write_b32 v66, v241 offset:29568\n"
      "s_branch L_end_11\n"
      "L_skip_10:\n"
      "v_mul_f32 v242, 2.0, v246\n"
      "v_mul_f32 v244, 0x44800000, v246\n"
      "v_pk_mul_f32 v[198:199], v[192:193], v[192:193] op_sel:[1,1] op_sel_hi:[1,0]\n"
      "v_mul_f32 v243, 2.0, v247\n"
      "v_mul_f32 v245, 0x44800000, v247\n"
      "v_pk_fma_f32 v[198:199], v[192:193], v[192:193], v[198:199] op_sel:[0,0,0] op_sel_hi:[0,1,1] neg_lo:[0,0,1]\n"
      "v_exp_f32 v242, v242\n"
      "v_exp_f32 v244, v244\n"
      "v_fract_f32 v243, v243\n"
      "v_fract_f32 v245, v245\n"
      "v_cos_f32 v194, v243\n"
      "v_cos_f32 v200, v245\n"
      "v_sin_f32 v195, v243\n"
      "v_sin_f32 v201, v245\n"
      "v_mul_f32 v194, v194, v242\n"
      "v_mul_f32 v200, v200, v244\n"
      "v_mul_f32 v195, v195, v242\n"
      "v_mul_f32 v201, v201, v244\n"
      "v_pk_mul_f32 v[202:203], v[8:9], v[198:199] op_sel:[1,1] op_sel_hi:[1,0]\n"
      "v_pk_mul_f32 v[196:197], v[194:195], v[194:195] op_sel:[1,1] op_sel_hi:[1,0]\n"
      "v_pk_fma_f32 v[202:203], v[8:9], v[198:199], v[202:203] op_sel:[0,0,0] op_sel_hi:[0,1,1] neg_lo:[0,0,1]\n"
      "v_pk_fma_f32 v[196:197], v[194:195], v[194:195], v[196:197] op_sel:[0,0,0] op_sel_hi:[0,1,1] neg_lo:[0,0,1]\n"
      "v_pk_mul_f32 v[206:207], v[8:9], v[200:201] op_sel:[1,1] op_sel_hi:[1,0]\n"
      "v_pk_fma_f32 v[206:207], v[8:9], v[200:201], v[206:207] op_sel:[0,0,0] op_sel_hi:[0,1,1] neg_lo:[0,0,1]\n"
      "v_cvt_pk_f16_f32 v238, v206, -v207\n"
      "ds_write_b32 v66, v238 offset:16896\n"
      "v_pk_mul_f32 v[204:205], v[202:203], v[192:193] op_sel:[1,1] op_sel_hi:[1,0]\n"
      "v_cvt_pk_f16_f32 v239, v202, -v203\n"
      "ds_write_b32 v66, v239 offset:8448\n"
      "v_pk_mul_f32 v[208:209], v[206:207], v[192:193] op_sel:[1,1] op_sel_hi:[1,0]\n"
      "v_pk_fma_f32 v[204:205], v[202:203], v[192:193], v[204:205] op_sel:[0,0,0] op_sel_hi:[0,1,1] neg_lo:[0,0,1]\n"
      "v_pk_mul_f32 v[210:211], v[202:203], v[194:195] op_sel:[1,1] op_sel_hi:[1,0]\n"
      "v_pk_fma_f32 v[208:209], v[206:207], v[192:193], v[208:209] op_sel:[0,0,0] op_sel_hi:[0,1,1] neg_lo:[0,0,1]\n"
      "v_pk_fma_f32 v[210:211], v[202:203], v[194:195], v[210:211] op_sel:[0,0,0] op_sel_hi:[0,1,1] neg_lo:[0,0,1]\n"
      "v_cvt_pk_f16_f32 v240, v210, -v211\n"
      "ds_write_b32 v66, v240 offset:8976\n"
      "v_pk_mul_f32 v[212:213], v[210:211], v[194:195] op_sel:[1,1] op_sel_hi:[1,0]\n"
      "v_pk_fma_f32 v[212:213], v[210:211], v[194:195], v[212:213] op_sel:[0,0,0] op_sel_hi:[0,1,1] neg_lo:[0,0,1]\n"
      "v_cvt_pk_f16_f32 v241, v212, -v213\n"
      "ds_write_b32 v66, v241 offset:9504\n"
      "v_pk_mul_f32 v[210:211], v[212:213], v[194:195] op_sel:[1,1] op_sel_hi:[1,0]\n"
      "v_pk_fma_f32 v[210:211], v[212:213], v[194:195], v[210:211] op_sel:[0,0,0] op_sel_hi:[0,1,1] neg_lo:[0,0,1]\n"
      "v_cvt_pk_f16_f32 v238, v210, -v211\n"
      "ds_write_b32 v66, v238 offset:10032\n"
      "s_add_u32 m0, s5, 0x4000\n"
      "s_nop 0\n"
      "global_load_lds_dwordx4 v77, s[6:7]\n"
      "v_cvt_pk_f16_f32 v239, v208, -v209\n"
      "ds_write_b32 v66, v239 offset:17424\n"
      "v_pk_mul_f32 v[202:203], v[204:205], v[192:193] op_sel:[1,1] op_sel_hi:[1,0]\n"
      "v_cvt_pk_f16_f32 v240, v204, -v205\n"
      "ds_write_b32 v66, v240 offset:10560\n"
      "v_pk_mul_f32 v[206:207], v[208:209], v[192:193] op_sel:[1,1] op_sel_hi:[1,0]\n"
      "v_pk_fma_f32 v[202:203], v[204:205], v[192:193], v[202:203] op_sel:[0,0,0] op_sel_hi:[0,1,1] neg_lo:[0,0,1]\n"
      "v_pk_mul_f32 v[210:211], v[204:205], v[194:195] op_sel:[1,1] op_sel_hi:[1,0]\n"
      "v_pk_fma_f32 v[206:207], v[208:209], v[192:193], v[206:207] op_sel:[0,0,0] op_sel_hi:[0,1,1] neg_lo:[0,0,1]\n"
      "v_pk_fma_f32 v[210:211], v[204:205], v[194:195], v[210:211] op_sel:[0,0,0] op_sel_hi:[0,1,1] neg_lo:[0,0,1]\n"
      "v_cvt_pk_f16_f32 v241, v210, -v211\n"
      "ds_write_b32 v66, v241 offset:11088\n"
      "v_pk_mul_f32 v[212:213], v[210:211], v[194:195] op_sel:[1,1] op_sel_hi:[1,0]\n"
      "v_pk_fma_f32 v[212:213], v[210:211], v[194:195], v[212:213] op_sel:[0,0,0] op_sel_hi:[0,1,1] neg_lo:[0,0,1]\n"
      "v_cvt_pk_f16_f32 v238, v212, -v213\n"
      "ds_write_b32 v66, v238 offset:11616\n"
      "v_pk_mul_f32 v[210:211], v[212:213], v[194:195] op_sel:[1,1] op_sel_hi:[1,0]\n"
      "v_pk_fma_f32 v[210:211], v[212:213], v[194:195], v[210:211] op_sel:[0,0,0] op_sel_hi:[0,1,1] neg_lo:[0,0,1]\n"
      "v_cvt_pk_f16_f32 v239, v210, -v211\n"
      "ds_write_b32 v66, v239 offset:12144\n"
      "v_cvt_pk_f16_f32 v240, v206, -v207\n"
      "ds_write_b32 v66, v240 offset:17952\n"
      "v_pk_mul_f32 v[204:205], v[202:203], v[192:193] op_sel:[1,1] op_sel_hi:[1,0]\n"
      "v_cvt_pk_f16_f32 v241, v202, -v203\n"
      "ds_write_b32 v66, v241 offset:12672\n"
      "v_pk_mul_f32 v[208:209], v[206:207], v[192:193] op_sel:[1,1] op_sel_hi:[1,0]\n"
      "v_pk_fma_f32 v[204:205], v[202:203], v[192:193], v[204:205] op_sel:[0,0,0] op_sel_hi:[0,1,1] neg_lo:[0,0,1]\n"
      "v_pk_mul_f32 v[210:211], v[202:203], v[196:197] op_sel:[1,1] op_sel_hi:[1,0]\n"
      "v_pk_fma_f32 v[208:209], v[206:207], v[192:193], v[208:209] op_sel:[0,0,0] op_sel_hi:[0,1,1] neg_lo:[0,0,1]\n"
      "v_pk_fma_f32 v[210:211], v[202:203], v[196:197], v[210:211] op_sel:[0,0,0] op_sel_hi:[0,1,1] neg_lo:[0,0,1]\n"
      "v_cvt_pk_f16_f32 v238, v210, -v211\n"
      "ds_write_b32 v66, v238 offset:13200\n"
      "s_add_u32 m0, s5, 0x6000\n"
      "s_nop 0\n"
      "global_load_lds_dwordx4 v78, s[6:7]\n"
      "v_cvt_pk_f16_f32 v239, v208, -v209\n"
      "ds_write_b32 v66, v239 offset:18480\n"
      "v_pk_mul_f32 v[202:203], v[204:205], v[192:193] op_sel:[1,1] op_sel_hi:[1,0]\n"
      "v_cvt_pk_f16_f32 v240, v204, -v205\n"
      "ds_write_b32 v66, v240 offset:13728\n"
      "v_pk_mul_f32 v[206:207], v[208:209], v[192:193] op_sel:[1,1] op_sel_hi:[1,0]\n"
      "v_pk_fma_f32 v[202:203], v[204:205], v[192:193], v[202:203] op_sel:[0,0,0] op_sel_hi:[0,1,1] neg_lo:[0,0,1]\n"
      "v_pk_mul_f32 v[210:211], v[204:205], v[196:197] op_sel:[1,1] op_sel_hi:[1,0]\n"
      "v_pk_fma_f32 v[206:207], v[208:209], v[192:193], v[206:207] op_sel:[0,0,0] op_sel_hi:[0,1,1] neg_lo:[0,0,1]\n"
      "v_pk_fma_f32 v[210:211], v[204:205], v[196:197], v[210:211] op_sel:[0,0,0] op_sel_hi:[0,1,1] neg_lo:[0,0,1]\n"
      "v_cvt_pk_f16_f32 v241, v210, -v211\n"
      "ds_write_b32 v66, v241 offset:14256\n"
      "v_cvt_pk_f16_f32 v238, v206, -v207\n"
      "ds_write_b32 v66, v238 offset:19008\n"
      "v_pk_mul_f32 v[204:205], v[202:203], v[192:193] op_sel:[1,1] op_sel_hi:[1,0]\n"
      "v_cvt_pk_f16_f32 v239, v202, -v203\n"
      "ds_write_b32 v66, v239 offset:14784\n"
      "v_pk_mul_f32 v[208:209], v[206:207], v[192:193] op_sel:[1,1] op_sel_hi:[1,0]\n"
      "v_pk_fma_f32 v[204:205], v[202:203], v[192:193], v[204:205] op_sel:[0,0,0] op_sel_hi:[0,1,1] neg_lo:[0,0,1]\n"
      "v_pk_mul_f32 v[210:211], v[202:203], v[196:197] op_sel:[1,1] op_sel_hi:[1,0]\n"
      "v_pk_fma_f32 v[208:209], v[206:207], v[192:193], v[208:209] op_sel:[0,0,0] op_sel_hi:[0,1,1] neg_lo:[0,0,1]\n"
      "v_pk_fma_f32 v[210:211], v[202:203], v[196:197], v[210:211] op_sel:[0,0,0] op_sel_hi:[0,1,1] neg_lo:[0,0,1]\n"
      "v_cvt_pk_f16_f32 v240, v210, -v211\n"
      "ds_write_b32 v66, v240 offset:15312\n"
      "s_add_u32 m0, s5, 0x8000\n"
      "s_nop 0\n"
      "global_load_lds_dwordx4 v79, s[6:7]\n"
      "v_cvt_pk_f16_f32 v241, v208, -v209\n"
      "ds_write_b32 v66, v241 offset:19536\n"
      "v_cvt_pk_f16_f32 v238, v204, -v205\n"
      "ds_write_b32 v66, v238 offset:15840\n"
      "v_pk_mul_f32 v[206:207], v[208:209], v[192:193] op_sel:[1,1] op_sel_hi:[1,0]\n"
      "v_pk_mul_f32 v[210:211], v[204:205], v[196:197] op_sel:[1,1] op_sel_hi:[1,0]\n"
      "v_pk_fma_f32 v[206:207], v[208:209], v[192:193], v[206:207] op_sel:[0,0,0] op_sel_hi:[0,1,1] neg_lo:[0,0,1]\n"
      "v_pk_fma_f32 v[210:211], v[204:205], v[196:197], v[210:211] op_sel:[0,0,0] op_sel_hi:[0,1,1] neg_lo:[0,0,1]\n"
      "v_cvt_pk_f16_f32 v239, v210, -v211\n"
      "ds_write_b32 v66, v239 offset:16368\n"
      "v_cvt_pk_f16_f32 v240, v206, -v207\n"
      "ds_write_b32 v66, v240 offset:20064\n"
      "v_pk_mul_f32 v[208:209], v[206:207], v[192:193] op_sel:[1,1] op_sel_hi:[1,0]\n"
      "v_pk_fma_f32 v[208:209], v[206:207], v[192:193], v[208:209] op_sel:[0,0,0] op_sel_hi:[0,1,1] neg_lo:[0,0,1]\n"
      "s_add_u32 m0, s5, 0xa000\n"
      "s_nop 0\n"
      "global_load_lds_dwordx4 v80, s[6:7]\n"
      "v_cvt_pk_f16_f32 v241, v208, -v209\n"
      "ds_write_b32 v66, v241 offset:20592\n"
      "v_pk_mul_f32 v[206:207], v[208:209], v[192:193] op_sel:[1,1] op_sel_hi:[1,0]\n"
      "v_pk_fma_f32 v[206:207], v[208:209], v[192:193], v[206:207] op_sel:[0,0,0] op_sel_hi:[0,1,1] neg_lo:[0,0,1]\n"
      "v_cvt_pk_f16_f32 v238, v206, -v207\n"
      "ds_write_b32 v66, v238 offset:21120\n"
      "L_end_11:\n"
      "s_waitcnt vmcnt(7)\n"
      "s_waitcnt lgkmcnt(0)\n"
      "s_barrier\n"
      "s_cmp_eq_u32 s33, 7\n"
      "s_cbranch_scc0 L_skip_12\n"
      "v_and_b32 v111, 7, v106\n"
      "v_lshl_add_u32 v110, v111, 2, s57\n"
      "ds_read_b32 v144, v110\n"
      "v_lshlrev_b32 v146, 3, v111\n"
      "v_mov_b32 v145, s38\n"
      "v_bfe_u32 v111, v106, 1, 3\n"
      "v_mul_u32_u24 v111, 0x1040, v111\n"
      "v_and_b32 v110, 16, v108\n"
      "v_add_u32 v111, v111, v110\n"
      "v_add_u32 v111, 0xf000, v111\n"
      "ds_write_b128 v111, v[38:41]\n"
      "L_skip_12:\n"
      "s_cmp_eq_u32 s33, 7\n"
      "s_cbranch_scc0 L_skip_13\n"
      "s_mov_b64 exec, 0xff\n"
      "s_waitcnt lgkmcnt(0)\n"
      "global_store_dwordx2 v146, v[144:145], s[10:11] sc1\n"
      "s_mov_b64 exec, -1\n"
      "L_skip_13:\n"
      "s_cmp_lt_u32 s4, 56\n"
      "s_cbranch_scc0 L_skip_14\n"
      "s_mov_b32 s26, 0x5040100\n"
      "s_mov_b32 s27, 0x7060302\n"
      "v_mov_b32 v128, v114\n"
      "v_mov_b32 v129, v115\n"
      "v_mov_b32 v130, v118\n"
      "v_mov_b32 v131, v119\n"
      "v_mov_b32 v132, v122\n"
      "v_mov_b32 v133, v123\n"
      "v_mov_b32 v134, v126\n"
      "v_mov_b32 v135, v127\n"
      "v_mov_b32_dpp v114, v112 row_ror:8 row_mask:0xf bank_mask:0x3\n"
      "v_mov_b32_dpp v115, v113 row_ror:8 row_mask:0xf bank_mask:0x3\n"
      "v_mov_b32_dpp v118, v116 row_ror:8 row_mask:0xf bank_mask:0x3\n"
      "v_mov_b32_dpp v119, v117 row_ror:8 row_mask:0xf bank_mask:0x3\n"
      "v_mov_b32_dpp v122, v120 row_ror:8 row_mask:0xf bank_mask:0x3\n"
      "v_mov_b32_dpp v123, v121 row_ror:8 row_mask:0xf bank_mask:0x3\n"
      "v_mov_b32_dpp v126, v124 row_ror:8 row_mask:0xf bank_mask:0x3\n"
      "v_mov_b32_dpp v127, v125 row_ror:8 row_mask:0xf bank_mask:0x3\n"
      "v_mov_b32_dpp v112, v128 row_ror:8 row_mask:0xf bank_mask:0xc\n"
      "v_mov_b32_dpp v113, v129 row_ror:8 row_mask:0xf bank_mask:0xc\n"
      "v_mov_b32_dpp v116, v130 row_ror:8 row_mask:0xf bank_mask:0xc\n"
      "v_mov_b32_dpp v117, v131 row_ror:8 row_mask:0xf bank_mask:0xc\n"
      "v_mov_b32_dpp v120, v132 row_ror:8 row_mask:0xf bank_mask:0xc\n"
      "v_mov_b32_dpp v121, v133 row_ror:8 row_mask:0xf bank_mask:0xc\n"
      "v_mov_b32_dpp v124, v134 row_ror:8 row_mask:0xf bank_mask:0xc\n"
      "v_mov_b32_dpp v125, v135 row_ror:8 row_mask:0xf bank_mask:0xc\n"
      "v_perm_b32 v128, v116, v112, s26\n"
      "v_perm_b32 v132, v116, v112, s27\n"
      "v_perm_b32 v136, v117, v113, s26\n"
      "v_perm_b32 v140, v117, v113, s27\n"
      "v_perm_b32 v129, v124, v120, s26\n"
      "v_perm_b32 v133, v124, v120, s27\n"
      "v_perm_b32 v137, v125, v121, s26\n"
      "v_perm_b32 v141, v125, v121, s27\n"
      "v_perm_b32 v130, v118, v114, s26\n"
      "v_perm_b32 v134, v118, v114, s27\n"
      "v_perm_b32 v138, v119, v115, s26\n"
      "v_perm_b32 v142, v119, v115, s27\n"
      "v_perm_b32 v131, v126, v122, s26\n"
      "v_perm_b32 v135, v126, v122, s27\n"
      "v_perm_b32 v139, v127, v123, s26\n"
      "v_perm_b32 v143, v127, v123, s27\n"
      "global_store_dwordx4 v108, v[128:131], s[8:9] offset:0 sc1\n"
      "global_store_dwordx4 v108, v[132:135], s[8:9] offset:1024 sc1\n"
      "global_store_dwordx4 v108, v[136:139], s[8:9] offset:2048 sc1\n"
      "global_store_dwordx4 v108, v[140:143], s[8:9] offset:3072 sc1\n"
      "L_skip_14:\n"
      "s_cmp_lt_u32 s33, 6\n"
      "s_cbranch_scc0 L_skip_15\n"
      "ds_read_b128 v[192:195], v65 offset:0\n"
      "ds_read_b128 v[2:5], v64 offset:21648\n"
      "ds_read_b128 v[196:199], v65 offset:64\n"
      "ds_read_b128 v[6:9], v64 offset:21712\n"
      "ds_read_b128 v[200:203], v65 offset:128\n"
      "ds_read_b128 v[10:13], v64 offset:21776\n"
      "ds_read_b128 v[204:207], v65 offset:192\n"
      "ds_read_b128 v[14:17], v64 offset:21840\n"
      "ds_read_b128 v[208:211], v65 offset:256\n"
      "ds_read_b128 v[212:215], v65 offset:320\n"
      "ds_read_b128 v[216:219], v65 offset:384\n"
      "ds_read_b128 v[220:223], v65 offset:448\n"
      "s_waitcnt lgkmcnt(10)\n"
      "v_mfma_f32_16x16x32_f16 v[224:227], v[192:195], v[2:5], 0\n"
      "ds_read_b128 v[2:5], v64 offset:21904\n"
      "s_waitcnt lgkmcnt(9)\n"
      "v_mfma_f32_16x16x32_f16 v[228:231], v[196:199], v[6:9], 0\n"
      "ds_read_b128 v[6:9], v64 offset:21968\n"
      "s_waitcnt lgkmcnt(8)\n"
      "v_mfma_f32_16x16x32_f16 v[224:227], v[200:203], v[10:13], v[224:227]\n"
      "ds_read_b128 v[10:13], v64 offset:22032\n"
      "s_waitcnt lgkmcnt(7)\n"
      "v_mfma_f32_16x16x32_f16 v[228:231], v[204:207], v[14:17], v[228:231]\n"
      "ds_read_b128 v[14:17], v64 offset:22096\n"
      "s_waitcnt lgkmcnt(3)\n"
      "v_mfma_f32_16x16x32_f16 v[224:227], v[208:211], v[2:5], v[224:227]\n"
      "s_waitcnt lgkmcnt(2)\n"
      "v_mfma_f32_16x16x32_f16 v[228:231], v[212:215], v[6:9], v[228:231]\n"
      "s_waitcnt lgkmcnt(1)\n"
      "v_mfma_f32_16x16x32_f16 v[224:227], v[216:219], v[10:13], v[224:227]\n"
      "s_waitcnt lgkmcnt(0)\n"
      "v_mfma_f32_16x16x32_f16 v[228:231], v[220:223], v[14:17], v[228:231]\n"
      "s_nop 7\n"
      "v_add_f32 v224, v224, v228\n"
      "v_add_f32 v225, v225, v229\n"
      "v_add_f32 v226, v226, v230\n"
      "v_add_f32 v227, v227, v231\n"
      "v_cvt_f16_f32 v224, v224\n"
      "v_cvt_f16_f32 v225, v225\n"
      "v_cvt_f16_f32 v226, v226\n"
      "v_cvt_f16_f32 v227, v227\n"
      "ds_write_b16 v29, v224\n"
      "ds_write_b16 v30, v225\n"
      "ds_write_b16 v31, v226\n"
      "ds_write_b16 v32, v227\n"
      "s_branch L_end_16\n"
      "L_skip_15:\n"
      "L_end_16:\n"
      "s_waitcnt lgkmcnt(0)\n"
      "s_barrier\n"
      "global_load_dwordx4 v[152:155], v63, s[30:31] sc1\n"
      "ds_read_u16 v245, v18\n"
      "ds_read_u16 v246, v19\n"
      "ds_read_u16 v247, v20\n"
      "ds_read_u16 v248, v21\n"
      "ds_read_u16 v249, v22\n"
      "ds_read_u16 v250, v23\n"
      "ds_read_u16 v251, v24\n"
      "ds_read_u16 v252, v25\n"
      "ds_read_u16 v253, v26\n"
      "ds_read_u16 v254, v27\n"
      "ds_read_u16 v255, v28\n"
      "v_and_b32 v111, 4, v106\n"
      "v_add_u32 v111, 0x27db0, v111\n"
      "ds_read_b32 v96, v111\n"
      "s_add_u32 s8, s6, 0\n"
      "s_addc_u32 s9, s7, 0\n"
      "s_add_u32 s6, s6, 0xe0000\n"
      "s_addc_u32 s7, s7, 0\n"
      "s_add_u32 m0, s5, 0x1c680\n"
      "s_nop 0\n"
      "global_load_lds_dwordx4 v77, s[6:7]\n"
      "s_waitcnt lgkmcnt(7)\n"
      "v_lshl_or_b32 v2, v249, 16, v248\n"
      "s_waitcnt lgkmcnt(5)\n"
      "v_lshl_or_b32 v3, v251, 16, v250\n"
      "s_waitcnt lgkmcnt(3)\n"
      "v_lshl_or_b32 v4, v253, 16, v252\n"
      "s_waitcnt lgkmcnt(1)\n"
      "v_lshl_or_b32 v5, v255, 16, v254\n"
      "ds_write_b128 v68, v[2:5] offset:0\n"
      "v_lshl_or_b32 v6, v248, 16, v247\n"
      "v_lshl_or_b32 v7, v250, 16, v249\n"
      "v_lshl_or_b32 v8, v252, 16, v251\n"
      "v_lshl_or_b32 v9, v254, 16, v253\n"
      "ds_write_b128 v68, v[6:9] offset:4160\n"
      "v_lshl_or_b32 v10, v247, 16, v246\n"
      "v_lshl_or_b32 v11, v249, 16, v248\n"
      "v_lshl_or_b32 v12, v251, 16, v250\n"
      "v_lshl_or_b32 v13, v253, 16, v252\n"
      "ds_write_b128 v68, v[10:13] offset:8320\n"
      "v_lshl_or_b32 v14, v246, 16, v245\n"
      "v_lshl_or_b32 v15, v248, 16, v247\n"
      "v_lshl_or_b32 v16, v250, 16, v249\n"
      "v_lshl_or_b32 v17, v252, 16, v251\n"
      "ds_write_b128 v68, v[14:17] offset:12480\n"
      "s_lshl1_add_u32 s43, s5, 0x10000\n"
      "s_add_u32 s24, s18, s43\n"
      "s_addc_u32 s25, s19, 0\n"
      "s_add_u32 s48, s18, s52\n"
      "s_addc_u32 s49, s19, 0\n"
      "L_ub_17:\n"
      "s_waitcnt vmcnt(1)\n"
      "v_xor_b32 v111, v152, v86\n"
      "v_bitop3_b32 v111, v111, v153, v87 bitop3:0xf6\n"
      "v_bitop3_b32 v111, v111, v154, v88 bitop3:0xf6\n"
      "v_bitop3_b32 v111, v111, v155, v89 bitop3:0xf6\n"
      "v_and_b32 v111, 0x30003, v111\n"
      "v_cmp_ne_u32 vcc, 0, v111\n"
      "s_nop 0\n"
      "s_cbranch_vccnz L_slow_19\n"
      "L_ua_18:\n"
      "v_and_b32 v152, 0xfffcfffc, v152\n"
      "v_and_b32 v153, 0xfffcfffc, v153\n"
      "v_and_b32 v154, 0xfffcfffc, v154\n"
      "v_and_b32 v155, 0xfffcfffc, v155\n"
      "ds_write_b128 v62, v[152:155]\n"
      "s_waitcnt lgkmcnt(0)\n"
      "s_barrier\n"
      "s_cmp_eq_u32 s42, 0\n"
      "s_cbranch_scc0 L_skip_2020\n"
      "ds_read_b64 v[160:161], v95 offset:3776\n"
      "ds_read_b64 v[162:163], v95 offset:3784\n"
      "ds_read_b128 v[164:167], v94 offset:3776\n"
      "ds_read_b128 v[168:171], v94 offset:3264\n"
      "ds_read_b128 v[172:175], v94 offset:704\n"
      "ds_read_b128 v[176:179], v94 offset:192\n"
      "ds_read_b64 v[180:181], v95 offset:3264\n"
      "ds_read_b64 v[182:183], v95 offset:3272\n"
      "ds_read_b128 v[184:187], v94 offset:2752\n"
      "ds_read_b64 v[188:189], v95 offset:2752\n"
      "ds_read_b64 v[190:191], v95 offset:2760\n"
      "ds_read_b128 v[192:195], v94 offset:2240\n"
      "s_waitcnt lgkmcnt(9)\n"
      "v_mfma_f32_16x16x32_f16 v[2:5], v[160:163], v[164:167], 0\n"
      "s_mov_b32 m0, s44\n"
      "s_nop 0\n"
      "global_load_lds_dwordx4 v108, s[24:25] offset:0 sc1\n"
      "s_add_u32 m0, s44, 0x1c00\n"
      "s_nop 0\n"
      "global_load_lds_dwordx4 v108, s[24:25] offset:1024 sc1\n"
      "ds_read_b64 v[164:165], v95 offset:2240\n"
      "ds_read_b64 v[166:167], v95 offset:2248\n"
      "s_waitcnt lgkmcnt(10)\n"
      "v_mfma_f32_16x16x32_f16 v[6:9], v[160:163], v[168:171], 0\n"
      "ds_read_b128 v[196:199], v94 offset:1728\n"
      "s_waitcnt lgkmcnt(10)\n"
      "v_mfma_f32_16x16x32_f16 v[10:13], v[160:163], v[172:175], 0\n"
      "s_add_u32 s20, s46, 0x38000\n"
      "s_addc_u32 s21, s47, 0\n"
      "global_store_dwordx4 v108, v[34:37], s[20:21] sc1\n"
      "s_add_u32 s20, s46, 0x54000\n"
      "s_addc_u32 s21, s47, 0\n"
      "global_store_dwordx4 v108, v[34:37], s[20:21] sc1\n"
      "ds_read_b64 v[200:201], v95 offset:1728\n"
      "ds_read_b64 v[202:203], v95 offset:1736\n"
      "s_waitcnt lgkmcnt(11)\n"
      "v_mfma_f32_16x16x32_f16 v[14:17], v[160:163], v[176:179], 0\n"
      "ds_read_b128 v[160:163], v94 offset:1216\n"
      "s_waitcnt lgkmcnt(10)\n"
      "v_mfma_f32_16x16x32_f16 v[2:5], v[180:183], v[168:171], v[2:5]\n"
      "ds_read_b64 v[168:169], v95 offset:1216\n"
      "ds_read_b64 v[170:171], v95 offset:1224\n"
      "s_waitcnt lgkmcnt(11)\n"
      "v_mfma_f32_16x16x32_f16 v[6:9], v[180:183], v[184:187], v[6:9]\n"
      "v_mfma_f32_16x16x32_f16 v[10:13], v[180:183], v[176:179], v[10:13]\n"
      "ds_read_b64 v[180:181], v95 offset:704\n"
      "ds_read_b64 v[182:183], v95 offset:712\n"
      "s_waitcnt lgkmcnt(11)\n"
      "v_mfma_f32_16x16x32_f16 v[2:5], v[188:191], v[184:187], v[2:5]\n"
      "s_waitcnt lgkmcnt(10)\n"
      "v_mfma_f32_16x16x32_f16 v[6:9], v[188:191], v[192:195], v[6:9]\n"
      "ds_read_b64 v[184:185], v95 offset:192\n"
      "ds_read_b64 v[186:187], v95 offset:200\n"
      "s_waitcnt lgkmcnt(10)\n"
      "v_mfma_f32_16x16x32_f16 v[2:5], v[164:167], v[192:195], v[2:5]\n"
      "s_add_u32 m0, s5, 0xc000\n"
      "s_nop 0\n"
      "global_load_lds_dwordx4 v81, s[8:9]\n"
      "ds_read_b64 v[188:189], v95 offset:3712\n"
      "ds_read_b64 v[190:191], v95 offset:3720\n"
      "ds_read_b128 v[192:195], v94 offset:3712\n"
      "s_waitcnt lgkmcnt(12)\n"
      "v_mfma_f32_16x16x32_f16 v[6:9], v[164:167], v[196:199], v[6:9]\n"
      "ds_read_b128 v[164:167], v94 offset:3200\n"
      "s_waitcnt lgkmcnt(11)\n"
      "v_mfma_f32_16x16x32_f16 v[2:5], v[200:203], v[196:199], v[2:5]\n"
      "ds_read_b128 v[196:199], v94 offset:640\n"
      "s_waitcnt lgkmcnt(11)\n"
      "v_mfma_f32_16x16x32_f16 v[6:9], v[200:203], v[160:163], v[6:9]\n"
      "ds_read_b128 v[200:203], v94 offset:128\n"
      "s_waitcnt lgkmcnt(10)\n"
      "v_mfma_f32_16x16x32_f16 v[2:5], v[168:171], v[160:163], v[2:5]\n"
      "ds_read_b64 v[160:161], v95 offset:3200\n"
      "ds_read_b64 v[162:163], v95 offset:3208\n"
      "v_mfma_f32_16x16x32_f16 v[6:9], v[168:171], v[172:175], v[6:9]\n"
      "ds_read_b128 v[168:171], v94 offset:2688\n"
      "s_waitcnt lgkmcnt(11)\n"
      "v_mfma_f32_16x16x32_f16 v[2:5], v[180:183], v[172:175], v[2:5]\n"
      "v_mfma_f32_16x16x32_f16 v[6:9], v[180:183], v[176:179], v[6:9]\n"
      "ds_read_b64 v[172:173], v95 offset:2688\n"
      "ds_read_b64 v[174:175], v95 offset:2696\n"
      "s_waitcnt lgkmcnt(11)\n"
      "v_mfma_f32_16x16x32_f16 v[2:5], v[184:187], v[176:179], v[2:5]\n"
      "ds_read_b128 v[176:179], v94 offset:2176\n"
      "s_waitcnt lgkmcnt(9)\n"
      "v_mfma_f32_16x16x32_f16 v[2:5], v[188:191], v[192:195], v[2:5]\n"
      "ds_read_b64 v[180:181], v95 offset:2176\n"
      "ds_read_b64 v[182:183], v95 offset:2184\n"
      "s_waitcnt lgkmcnt(10)\n"
      "v_mfma_f32_16x16x32_f16 v[6:9], v[188:191], v[164:167], v[6:9]\n"
      "ds_read_b128 v[184:187], v94 offset:1664\n"
      "s_waitcnt lgkmcnt(10)\n"
      "v_mfma_f32_16x16x32_f16 v[10:13], v[188:191], v[196:199], v[10:13]\n"
      "ds_read_b64 v[192:193], v95 offset:1664\n"
      "ds_read_b64 v[194:195], v95 offset:1672\n"
      "s_waitcnt lgkmcnt(11)\n"
      "v_mfma_f32_16x16x32_f16 v[14:17], v[188:191], v[200:203], v[14:17]\n"
      "ds_read_b128 v[188:191], v94 offset:1152\n"
      "s_waitcnt lgkmcnt(10)\n"
      "v_mfma_f32_16x16x32_f16 v[2:5], v[160:163], v[164:167], v[2:5]\n"
      "ds_read_b64 v[164:165], v95 offset:1152\n"
      "ds_read_b64 v[166:167], v95 offset:1160\n"
      "s_waitcnt lgkmcnt(11)\n"
      "v_mfma_f32_16x16x32_f16 v[6:9], v[160:163], v[168:171], v[6:9]\n"
      "s_add_u32 m0, s5, 0x1e680\n"
      "s_nop 0\n"
      "global_load_lds_dwordx4 v78, s[6:7]\n"
      "v_mfma_f32_16x16x32_f16 v[10:13], v[160:163], v[200:203], v[10:13]\n"
      "ds_read_b64 v[160:161], v95 offset:640\n"
      "ds_read_b64 v[162:163], v95 offset:648\n"
      "s_waitcnt lgkmcnt(11)\n"
      "v_mfma_f32_16x16x32_f16 v[2:5], v[172:175], v[168:171], v[2:5]\n"
      "s_waitcnt lgkmcnt(10)\n"
      "v_mfma_f32_16x16x32_f16 v[6:9], v[172:175], v[176:179], v[6:9]\n"
      "s_waitcnt vmcnt(4)\n"
      "ds_read_b128 v[38:41], v91 offset:0\n"
      "ds_read_b128 v[42:45], v91 offset:16\n"
      "ds_read_b128 v[46:49], v91 offset:32\n"
      "ds_read_b128 v[50:53], v91 offset:48\n"
      "ds_read_b128 v[54:57], v92\n"
      "ds_read_b64 v[168:169], v95 offset:128\n"
      "ds_read_b64 v[170:171], v95 offset:136\n"
      "s_waitcnt lgkmcnt(15)\n"
      "v_mfma_f32_16x16x32_f16 v[2:5], v[180:183], v[176:179], v[2:5]\n"
      "ds_read_b64 v[172:173], v95 offset:3648\n"
      "ds_read_b64 v[174:175], v95 offset:3656\n"
      "ds_read_b128 v[176:179], v94 offset:3648\n"
      "s_waitcnt lgkmcnt(15)\n"
      "v_mfma_f32_16x16x32_f16 v[6:9], v[180:183], v[184:187], v[6:9]\n"
      "ds_read_b128 v[180:183], v94 offset:3136\n"
      "v_mfma_f32_16x16x32_f16 v[2:5], v[192:195], v[184:187], v[2:5]\n"
      "ds_read_b128 v[184:187], v94 offset:576\n"
      "s_waitcnt lgkmcnt(15)\n"
      "v_mfma_f32_16x16x32_f16 v[6:9], v[192:195], v[188:191], v[6:9]\n"
      "s_waitcnt lgkmcnt(11)\n"
      "v_xor_b32 v109, s38, v39\n"
      "v_bitop3_b32 v109, v109, v41, s38 bitop3:0xf6\n"
      "s_add_u32 m0, s5, 0x20680\n"
      "s_nop 0\n"
      "global_load_lds_dwordx4 v79, s[6:7]\n"
      "ds_read_b128 v[192:195], v94 offset:64\n"
      "v_mfma_f32_16x16x32_f16 v[2:5], v[164:167], v[188:191], v[2:5]\n"
      "s_waitcnt lgkmcnt(11)\n"
      "v_bitop3_b32 v109, v109, v43, s38 bitop3:0xf6\n"
      "v_bitop3_b32 v109, v109, v45, s38 bitop3:0xf6\n"
      "ds_read_b64 v[188:189], v95 offset:3136\n"
      "ds_read_b64 v[190:191], v95 offset:3144\n"
      "v_mfma_f32_16x16x32_f16 v[6:9], v[164:167], v[196:199], v[6:9]\n"
      "s_waitcnt lgkmcnt(12)\n"
      "v_bitop3_b32 v109, v109, v47, s38 bitop3:0xf6\n"
      "v_bitop3_b32 v109, v109, v49, s38 bitop3:0xf6\n"
      "ds_read_b128 v[164:167], v94 offset:2624\n"
      "v_mfma_f32_16x16x32_f16 v[2:5], v[160:163], v[196:199], v[2:5]\n"
      "s_waitcnt lgkmcnt(12)\n"
      "v_bitop3_b32 v109, v109, v51, s38 bitop3:0xf6\n"
      "v_bitop3_b32 v109, v109, v53, s38 bitop3:0xf6\n"
      "v_mfma_f32_16x16x32_f16 v[6:9], v[160:163], v[200:203], v[6:9]\n"
      "v_cmp_ne_u32 vcc, 0, v109\n"
      "s_nop 0\n"
      "s_cbranch_vccnz L_slow_2023\n"
      "L_sa_2022:\n"
      "ds_read_b64 v[160:161], v95 offset:2624\n"
      "ds_read_b64 v[162:163], v95 offset:2632\n"
      "s_waitcnt lgkmcnt(11)\n"
      "v_mfma_f32_16x16x32_f16 v[2:5], v[168:171], v[200:203], v[2:5]\n"
      "v_and_b32 v111, 0xffff0000, v38\n"
      "ds_read_b128 v[168:171], v94 offset:2112\n"
      "s_waitcnt lgkmcnt(9)\n"
      "v_mfma_f32_16x16x32_f16 v[2:5], v[172:175], v[176:179], v[2:5]\n"
      "v_lshlrev_b32 v38, 16, v38\n"
      "ds_read_b64 v[176:177], v95 offset:2112\n"
      "ds_read_b64 v[178:179], v95 offset:2120\n"
      "s_waitcnt lgkmcnt(10)\n"
      "v_mfma_f32_16x16x32_f16 v[6:9], v[172:175], v[180:183], v[6:9]\n"
      "v_add_f32 v111, v111, v90\n"
      "s_add_u32 m0, s5, 0x22680\n"
      "s_nop 0\n"
      "global_load_lds_dwordx4 v80, s[6:7]\n"
      "ds_read_b128 v[196:199], v94 offset:1600\n"
      "s_waitcnt lgkmcnt(10)\n"
      "v_mfma_f32_16x16x32_f16 v[10:13], v[172:175], v[184:187], v[10:13]\n"
      "v_fma_mix_f32 v82, v54, v111, v38 op_sel:[0,0,0] op_sel_hi:[1,0,0]\n"
      "ds_read_b64 v[200:201], v95 offset:1600\n"
      "ds_read_b64 v[202:203], v95 offset:1608\n"
      "s_waitcnt lgkmcnt(11)\n"
      "v_mfma_f32_16x16x32_f16 v[14:17], v[172:175], v[192:195], v[14:17]\n"
      "v_and_b32 v111, 0xffff0000, v40\n"
      "ds_read_b128 v[172:175], v94 offset:1088\n"
      "s_waitcnt lgkmcnt(10)\n"
      "v_mfma_f32_16x16x32_f16 v[2:5], v[188:191], v[180:183], v[2:5]\n"
      "v_lshlrev_b32 v40, 16, v40\n"
      "ds_read_b64 v[180:181], v95 offset:1088\n"
      "ds_read_b64 v[182:183], v95 offset:1096\n"
      "s_waitcnt lgkmcnt(11)\n"
      "v_mfma_f32_16x16x32_f16 v[6:9], v[188:191], v[164:167], v[6:9]\n"
      "v_add_f32 v111, v111, v90\n"
      "v_mfma_f32_16x16x32_f16 v[10:13], v[188:191], v[192:195], v[10:13]\n"
      "v_fma_mix_f32 v83, v54, v111, v40 op_sel:[1,0,0] op_sel_hi:[1,0,0]\n"
      "ds_read_b64 v[188:189], v95 offset:576\n"
      "ds_read_b64 v[190:191], v95 offset:584\n"
      "s_waitcnt lgkmcnt(11)\n"
      "v_mfma_f32_16x16x32_f16 v[2:5], v[160:163], v[164:167], v[2:5]\n"
      "v_and_b32 v111, 0xffff0000, v42\n"
      "s_waitcnt lgkmcnt(10)\n"
      "v_mfma_f32_16x16x32_f16 v[6:9], v[160:163], v[168:171], v[6:9]\n"
      "v_lshlrev_b32 v42, 16, v42\n"
      "s_add_u32 m0, s5, 0x24680\n"
      "s_nop 0\n"
      "global_load_lds_dwordx4 v81, s[6:7]\n"
      "ds_read_b64 v[160:161], v95 offset:64\n"
      "ds_read_b64 v[162:163], v95 offset:72\n"
      "s_waitcnt lgkmcnt(10)\n"
      "v_mfma_f32_16x16x32_f16 v[2:5], v[176:179], v[168:171], v[2:5]\n"
      "v_add_f32 v111, v111, v90\n"
      "ds_read_b64 v[164:165], v95 offset:3584\n"
      "ds_read_b64 v[166:167], v95 offset:3592\n"
      "ds_read_b128 v[168:171], v94 offset:3584\n"
      "s_waitcnt lgkmcnt(12)\n"
      "v_mfma_f32_16x16x32_f16 v[6:9], v[176:179], v[196:199], v[6:9]\n"
      "v_fma_mix_f32 v84, v55, v111, v42 op_sel:[0,0,0] op_sel_hi:[1,0,0]\n"
      "ds_read_b128 v[176:179], v94 offset:3072\n"
      "s_waitcnt lgkmcnt(11)\n"
      "v_mfma_f32_16x16x32_f16 v[2:5], v[200:203], v[196:199], v[2:5]\n"
      "v_and_b32 v111, 0xffff0000, v44\n"
      "ds_read_b128 v[196:199], v94 offset:512\n"
      "s_waitcnt lgkmcnt(11)\n"
      "v_mfma_f32_16x16x32_f16 v[6:9], v[200:203], v[172:175], v[6:9]\n"
      "v_lshlrev_b32 v44, 16, v44\n"
      "ds_read_b128 v[200:203], v94 offset:0\n"
      "s_waitcnt lgkmcnt(10)\n"
      "v_mfma_f32_16x16x32_f16 v[2:5], v[180:183], v[172:175], v[2:5]\n"
      "v_add_f32 v111, v111, v90\n"
      "ds_read_b64 v[172:173], v95 offset:3072\n"
      "ds_read_b64 v[174:175], v95 offset:3080\n"
      "v_mfma_f32_16x16x32_f16 v[6:9], v[180:183], v[184:187], v[6:9]\n"
      "v_fma_mix_f32 v85, v55, v111, v44 op_sel:[1,0,0] op_sel_hi:[1,0,0]\n"
      "ds_read_b128 v[180:183], v94 offset:2560\n"
      "s_waitcnt lgkmcnt(11)\n"
      "v_mfma_f32_16x16x32_f16 v[2:5], v[188:191], v[184:187], v[2:5]\n"
      "v_mfma_f32_16x16x32_f16 v[6:9], v[188:191], v[192:195], v[6:9]\n"
      "v_and_b32 v111, 0xffff0000, v46\n"
      "ds_read_b64 v[184:185], v95 offset:2560\n"
      "ds_read_b64 v[186:187], v95 offset:2568\n"
      "s_waitcnt lgkmcnt(11)\n"
      "v_mfma_f32_16x16x32_f16 v[2:5], v[160:163], v[192:195], v[2:5]\n"
      "v_lshlrev_b32 v46, 16, v46\n"
      "ds_read_b128 v[160:163], v94 offset:2048\n"
      "s_waitcnt lgkmcnt(9)\n"
      "v_mfma_f32_16x16x32_f16 v[2:5], v[164:167], v[168:171], v[2:5]\n"
      "v_add_f32 v111, v111, v90\n"
      "s_add_u32 m0, s5, 0x18680\n"
      "s_nop 0\n"
      "global_load_lds_dwordx4 v75, s[6:7]\n"
      "ds_read_b64 v[168:169], v95 offset:2048\n"
      "ds_read_b64 v[170:171], v95 offset:2056\n"
      "s_waitcnt lgkmcnt(10)\n"
      "v_mfma_f32_16x16x32_f16 v[6:9], v[164:167], v[176:179], v[6:9]\n"
      "v_fma_mix_f32 v86, v56, v111, v46 op_sel:[0,0,0] op_sel_hi:[1,0,0]\n"
      "ds_read_b128 v[188:191], v94 offset:1536\n"
      "s_waitcnt lgkmcnt(10)\n"
      "v_mfma_f32_16x16x32_f16 v[10:13], v[164:167], v[196:199], v[10:13]\n"
      "v_and_b32 v111, 0xffff0000, v48\n"
      "ds_read_b64 v[192:193], v95 offset:1536\n"
      "ds_read_b64 v[194:195], v95 offset:1544\n"
      "s_waitcnt lgkmcnt(11)\n"
      "v_mfma_f32_16x16x32_f16 v[14:17], v[164:167], v[200:203], v[14:17]\n"
      "v_lshlrev_b32 v48, 16, v48\n"
      "s_add_u32 m0, s5, 0x1a680\n"
      "s_nop 0\n"
      "global_load_lds_dwordx4 v76, s[6:7]\n"
      "ds_read_b128 v[164:167], v94 offset:1024\n"
      "s_waitcnt lgkmcnt(10)\n"
      "v_mfma_f32_16x16x32_f16 v[2:5], v[172:175], v[176:179], v[2:5]\n"
      "v_add_f32 v111, v111, v90\n"
      "ds_read_b64 v[176:177], v95 offset:1024\n"
      "ds_read_b64 v[178:179], v95 offset:1032\n"
      "s_waitcnt lgkmcnt(11)\n"
      "v_mfma_f32_16x16x32_f16 v[6:9], v[172:175], v[180:183], v[6:9]\n"
      "v_fma_mix_f32 v87, v56, v111, v48 op_sel:[1,0,0] op_sel_hi:[1,0,0]\n"
      "v_mfma_f32_16x16x32_f16 v[10:13], v[172:175], v[200:203], v[10:13]\n"
      "v_and_b32 v111, 0xffff0000, v50\n"
      "ds_read_b64 v[172:173], v95 offset:512\n"
      "ds_read_b64 v[174:175], v95 offset:520\n"
      "s_waitcnt lgkmcnt(11)\n"
      "v_mfma_f32_16x16x32_f16 v[2:5], v[184:187], v[180:183], v[2:5]\n"
      "v_lshlrev_b32 v50, 16, v50\n"
      "s_waitcnt lgkmcnt(10)\n"
      "v_mfma_f32_16x16x32_f16 v[6:9], v[184:187], v[160:163], v[6:9]\n"
      "v_add_f32 v111, v111, v90\n"
      "ds_read_b64 v[180:181], v95 offset:0\n"
      "ds_read_b64 v[182:183], v95 offset:8\n"
      "s_waitcnt lgkmcnt(10)\n"
      "v_mfma_f32_16x16x32_f16 v[2:5], v[168:171], v[160:163], v[2:5]\n"
      "v_fma_mix_f32 v88, v57, v111, v50 op_sel:[0,0,0] op_sel_hi:[1,0,0]\n"
      "s_waitcnt lgkmcnt(9)\n"
      "v_mfma_f32_16x16x32_f16 v[6:9], v[168:171], v[188:191], v[6:9]\n"
      "v_and_b32 v111, 0xffff0000, v52\n"
      "s_waitcnt lgkmcnt(7)\n"
      "v_mfma_f32_16x16x32_f16 v[2:5], v[192:195], v[188:191], v[2:5]\n"
      "v_lshlrev_b32 v52, 16, v52\n"
      "s_waitcnt lgkmcnt(6)\n"
      "v_mfma_f32_16x16x32_f16 v[6:9], v[192:195], v[164:167], v[6:9]\n"
      "v_add_f32 v111, v111, v90\n"
      "s_waitcnt lgkmcnt(4)\n"
      "v_mfma_f32_16x16x32_f16 v[2:5], v[176:179], v[164:167], v[2:5]\n"
      "v_fma_mix_f32 v89, v57, v111, v52 op_sel:[1,0,0] op_sel_hi:[1,0,0]\n"
      "v_mfma_f32_16x16x32_f16 v[6:9], v[176:179], v[196:199], v[6:9]\n"
      "s_waitcnt lgkmcnt(2)\n"
      "v_mfma_f32_16x16x32_f16 v[2:5], v[172:175], v[196:199], v[2:5]\n"
      "v_mfma_f32_16x16x32_f16 v[6:9], v[172:175], v[200:203], v[6:9]\n"
      "s_waitcnt lgkmcnt(0)\n"
      "v_mfma_f32_16x16x32_f16 v[2:5], v[180:183], v[200:203], v[2:5]\n"
      "s_barrier\n"
      "s_nop 6\n"
      "ds_write_b128 v93, v[2:5] offset:8960\n"
      "ds_write_b128 v93, v[6:9] offset:7680\n"
      "ds_write_b128 v93, v[10:13] offset:1280\n"
      "ds_write_b128 v93, v[14:17] offset:0\n"
      "s_waitcnt vmcnt(7)\n"
      "s_branch L_end_4024\n"
      "L_skip_2020:\n"
      "ds_read_b64 v[160:161], v95 offset:3776\n"
      "ds_read_b64 v[162:163], v95 offset:3784\n"
      "ds_read_b128 v[164:167], v94 offset:2752\n"
      "ds_read_b128 v[168:171], v94 offset:2240\n"
      "ds_read_b128 v[172:175], v94 offset:1728\n"
      "ds_read_b128 v[176:179], v94 offset:1216\n"
      "ds_read_b64 v[180:181], v95 offset:3264\n"
      "ds_read_b64 v[182:183], v95 offset:3272\n"
      "ds_read_b128 v[184:187], v94 offset:704\n"
      "ds_read_b64 v[188:189], v95 offset:2752\n"
      "ds_read_b64 v[190:191], v95 offset:2760\n"
      "s_waitcnt lgkmcnt(8)\n"
      "v_mfma_f32_16x16x32_f16 v[2:5], v[160:163], v[164:167], 0\n"
      "s_mov_b32 m0, s44\n"
      "s_nop 0\n"
      "global_load_lds_dwordx4 v108, s[24:25] offset:0 sc1\n"
      "s_add_u32 m0, s44, 0x1c00\n"
      "s_nop 0\n"
      "global_load_lds_dwordx4 v108, s[24:25] offset:1024 sc1\n"
      "s_waitcnt lgkmcnt(7)\n"
      "v_mfma_f32_16x16x32_f16 v[6:9], v[160:163], v[168:171], 0\n"
      "s_waitcnt lgkmcnt(6)\n"
      "v_mfma_f32_16x16x32_f16 v[10:13], v[160:163], v[172:175], 0\n"
      "s_add_u32 s20, s46, 0x38000\n"
      "s_addc_u32 s21, s47, 0\n"
      "s_cmp_eq_u32 s33, 7\n"
      "s_cbranch_scc1 L_skip_4027\n"
      "global_store_dwordx4 v108, v[34:37], s[20:21] sc1\n"
      "L_skip_4027:\n"
      "s_add_u32 s20, s46, 0x54000\n"
      "s_addc_u32 s21, s47, 0\n"
      "s_cmp_eq_u32 s33, 7\n"
      "s_cbranch_scc1 L_skip_4028\n"
      "global_store_dwordx4 v108, v[34:37], s[20:21] sc1\n"
      "L_skip_4028:\n"
      "ds_read_b128 v[164:167], v94 offset:192\n"
      "s_waitcnt lgkmcnt(6)\n"
      "v_mfma_f32_16x16x32_f16 v[14:17], v[160:163], v[176:179], 0\n"
      "ds_read_b64 v[160:161], v95 offset:2240\n"
      "ds_read_b64 v[162:163], v95 offset:2248\n"
      "s_waitcnt lgkmcnt(6)\n"
      "v_mfma_f32_16x16x32_f16 v[2:5], v[180:183], v[168:171], v[2:5]\n"
      "v_mfma_f32_16x16x32_f16 v[6:9], v[180:183], v[172:175], v[6:9]\n"
      "v_mfma_f32_16x16x32_f16 v[10:13], v[180:183], v[176:179], v[10:13]\n"
      "ds_read_b64 v[168:169], v95 offset:1728\n"
      "ds_read_b64 v[170:171], v95 offset:1736\n"
      "s_waitcnt lgkmcnt(7)\n"
      "v_mfma_f32_16x16x32_f16 v[14:17], v[180:183], v[184:187], v[14:17]\n"
      "s_waitcnt lgkmcnt(5)\n"
      "v_mfma_f32_16x16x32_f16 v[2:5], v[188:191], v[172:175], v[2:5]\n"
      "ds_read_b64 v[172:173], v95 offset:1216\n"
      "ds_read_b64 v[174:175], v95 offset:1224\n"
      "v_mfma_f32_16x16x32_f16 v[6:9], v[188:191], v[176:179], v[6:9]\n"
      "s_add_u32 m0, s5, 0xc000\n"
      "s_nop 0\n"
      "global_load_lds_dwordx4 v81, s[8:9]\n"
      "ds_read_b64 v[180:181], v95 offset:3712\n"
      "ds_read_b64 v[182:183], v95 offset:3720\n"
      "ds_read_b128 v[192:195], v94 offset:2688\n"
      "v_mfma_f32_16x16x32_f16 v[10:13], v[188:191], v[184:187], v[10:13]\n"
      "ds_read_b128 v[196:199], v94 offset:2176\n"
      "s_waitcnt lgkmcnt(10)\n"
      "v_mfma_f32_16x16x32_f16 v[14:17], v[188:191], v[164:167], v[14:17]\n"
      "ds_read_b128 v[188:191], v94 offset:1664\n"
      "s_waitcnt lgkmcnt(9)\n"
      "v_mfma_f32_16x16x32_f16 v[2:5], v[160:163], v[176:179], v[2:5]\n"
      "ds_read_b128 v[176:179], v94 offset:1152\n"
      "v_mfma_f32_16x16x32_f16 v[6:9], v[160:163], v[184:187], v[6:9]\n"
      "ds_read_b64 v[200:201], v95 offset:3200\n"
      "ds_read_b64 v[202:203], v95 offset:3208\n"
      "v_mfma_f32_16x16x32_f16 v[10:13], v[160:163], v[164:167], v[10:13]\n"
      "s_waitcnt lgkmcnt(10)\n"
      "v_mfma_f32_16x16x32_f16 v[2:5], v[168:171], v[184:187], v[2:5]\n"
      "v_mfma_f32_16x16x32_f16 v[6:9], v[168:171], v[164:167], v[6:9]\n"
      "ds_read_b128 v[160:163], v94 offset:640\n"
      "s_waitcnt lgkmcnt(9)\n"
      "v_mfma_f32_16x16x32_f16 v[2:5], v[172:175], v[164:167], v[2:5]\n"
      "ds_read_b64 v[164:165], v95 offset:2688\n"
      "ds_read_b64 v[166:167], v95 offset:2696\n"
      "s_waitcnt lgkmcnt(8)\n"
      "v_mfma_f32_16x16x32_f16 v[2:5], v[180:183], v[192:195], v[2:5]\n"
      "s_waitcnt lgkmcnt(7)\n"
      "v_mfma_f32_16x16x32_f16 v[6:9], v[180:183], v[196:199], v[6:9]\n"
      "s_waitcnt lgkmcnt(6)\n"
      "v_mfma_f32_16x16x32_f16 v[10:13], v[180:183], v[188:191], v[10:13]\n"
      "ds_read_b128 v[168:171], v94 offset:128\n"
      "s_waitcnt lgkmcnt(6)\n"
      "v_mfma_f32_16x16x32_f16 v[14:17], v[180:183], v[176:179], v[14:17]\n"
      "ds_read_b64 v[172:173], v95 offset:2176\n"
      "ds_read_b64 v[174:175], v95 offset:2184\n"
      "s_waitcnt lgkmcnt(6)\n"
      "v_mfma_f32_16x16x32_f16 v[2:5], v[200:203], v[196:199], v[2:5]\n"
      "v_mfma_f32_16x16x32_f16 v[6:9], v[200:203], v[188:191], v[6:9]\n"
      "s_add_u32 m0, s5, 0x1e680\n"
      "s_nop 0\n"
      "global_load_lds_dwordx4 v78, s[6:7]\n"
      "v_mfma_f32_16x16x32_f16 v[10:13], v[200:203], v[176:179], v[10:13]\n"
      "ds_read_b64 v[180:181], v95 offset:1664\n"
      "ds_read_b64 v[182:183], v95 offset:1672\n"
      "s_waitcnt lgkmcnt(7)\n"
      "v_mfma_f32_16x16x32_f16 v[14:17], v[200:203], v[160:163], v[14:17]\n"
      "s_waitcnt lgkmcnt(5)\n"
      "v_mfma_f32_16x16x32_f16 v[2:5], v[164:167], v[188:191], v[2:5]\n"
      "s_waitcnt vmcnt(2)\n"
      "ds_read_b128 v[38:41], v91 offset:0\n"
      "ds_read_b128 v[42:45], v91 offset:16\n"
      "ds_read_b128 v[46:49], v91 offset:32\n"
      "ds_read_b128 v[50:53], v91 offset:48\n"
      "ds_read_b128 v[54:57], v92\n"
      "ds_read_b64 v[184:185], v95 offset:1152\n"
      "ds_read_b64 v[186:187], v95 offset:1160\n"
      "v_mfma_f32_16x16x32_f16 v[6:9], v[164:167], v[176:179], v[6:9]\n"
      "ds_read_b64 v[188:189], v95 offset:3648\n"
      "ds_read_b64 v[190:191], v95 offset:3656\n"
      "ds_read_b128 v[192:195], v94 offset:2624\n"
      "v_mfma_f32_16x16x32_f16 v[10:13], v[164:167], v[160:163], v[10:13]\n"
      "ds_read_b128 v[196:199], v94 offset:2112\n"
      "s_waitcnt lgkmcnt(15)\n"
      "v_mfma_f32_16x16x32_f16 v[14:17], v[164:167], v[168:171], v[14:17]\n"
      "ds_read_b128 v[164:167], v94 offset:1600\n"
      "s_waitcnt lgkmcnt(14)\n"
      "v_mfma_f32_16x16x32_f16 v[2:5], v[172:175], v[176:179], v[2:5]\n"
      "s_waitcnt lgkmcnt(11)\n"
      "v_xor_b32 v109, s38, v39\n"
      "v_bitop3_b32 v109, v109, v41, s38 bitop3:0xf6\n"
      "s_add_u32 m0, s5, 0x20680\n"
      "s_nop 0\n"
      "global_load_lds_dwordx4 v79, s[6:7]\n"
      "ds_read_b128 v[176:179], v94 offset:1088\n"
      "v_mfma_f32_16x16x32_f16 v[6:9], v[172:175], v[160:163], v[6:9]\n"
      "s_waitcnt lgkmcnt(11)\n"
      "v_bitop3_b32 v109, v109, v43, s38 bitop3:0xf6\n"
      "v_bitop3_b32 v109, v109, v45, s38 bitop3:0xf6\n"
      "ds_read_b64 v[200:201], v95 offset:3136\n"
      "ds_read_b64 v[202:203], v95 offset:3144\n"
      "v_mfma_f32_16x16x32_f16 v[10:13], v[172:175], v[168:171], v[10:13]\n"
      "s_waitcnt lgkmcnt(12)\n"
      "v_bitop3_b32 v109, v109, v47, s38 bitop3:0xf6\n"
      "v_bitop3_b32 v109, v109, v49, s38 bitop3:0xf6\n"
      "v_mfma_f32_16x16x32_f16 v[2:5], v[180:183], v[160:163], v[2:5]\n"
      "s_waitcnt lgkmcnt(11)\n"
      "v_bitop3_b32 v109, v109, v51, s38 bitop3:0xf6\n"
      "v_bitop3_b32 v109, v109, v53, s38 bitop3:0xf6\n"
      "v_mfma_f32_16x16x32_f16 v[6:9], v[180:183], v[168:171], v[6:9]\n"
      "v_cmp_ne_u32 vcc, 0, v109\n"
      "s_nop 0\n"
      "s_cbranch_vccnz L_slow_4029\n"
      "L_sa_4026:\n"
      "ds_read_b128 v[160:163], v94 offset:576\n"
      "s_waitcnt lgkmcnt(9)\n"
      "v_mfma_f32_16x16x32_f16 v[2:5], v[184:187], v[168:171], v[2:5]\n"
      "v_and_b32 v111, 0xffff0000, v38\n"
      "ds_read_b64 v[168:169], v95 offset:2624\n"
      "ds_read_b64 v[170:171], v95 offset:2632\n"
      "s_waitcnt lgkmcnt(8)\n"
      "v_mfma_f32_16x16x32_f16 v[2:5], v[188:191], v[192:195], v[2:5]\n"
      "v_lshlrev_b32 v38, 16, v38\n"
      "s_waitcnt lgkmcnt(7)\n"
      "v_mfma_f32_16x16x32_f16 v[6:9], v[188:191], v[196:199], v[6:9]\n"
      "v_add_f32 v111, v111, v90\n"
      "s_add_u32 m0, s5, 0x22680\n"
      "s_nop 0\n"
      "global_load_lds_dwordx4 v80, s[6:7]\n"
      "s_waitcnt lgkmcnt(6)\n"
      "v_mfma_f32_16x16x32_f16 v[10:13], v[188:191], v[164:167], v[10:13]\n"
      "v_fma_mix_f32 v82, v54, v111, v38 op_sel:[0,0,0] op_sel_hi:[1,0,0]\n"
      "ds_read_b128 v[172:175], v94 offset:64\n"
      "s_waitcnt lgkmcnt(6)\n"
      "v_mfma_f32_16x16x32_f16 v[14:17], v[188:191], v[176:179], v[14:17]\n"
      "v_and_b32 v111, 0xffff0000, v40\n"
      "ds_read_b64 v[180:181], v95 offset:2112\n"
      "ds_read_b64 v[182:183], v95 offset:2120\n"
      "s_waitcnt lgkmcnt(6)\n"
      "v_mfma_f32_16x16x32_f16 v[2:5], v[200:203], v[196:199], v[2:5]\n"
      "v_lshlrev_b32 v40, 16, v40\n"
      "v_mfma_f32_16x16x32_f16 v[6:9], v[200:203], v[164:167], v[6:9]\n"
      "v_add_f32 v111, v111, v90\n"
      "v_mfma_f32_16x16x32_f16 v[10:13], v[200:203], v[176:179], v[10:13]\n"
      "v_fma_mix_f32 v83, v54, v111, v40 op_sel:[1,0,0] op_sel_hi:[1,0,0]\n"
      "ds_read_b64 v[184:185], v95 offset:1600\n"
      "ds_read_b64 v[186:187], v95 offset:1608\n"
      "s_waitcnt lgkmcnt(7)\n"
      "v_mfma_f32_16x16x32_f16 v[14:17], v[200:203], v[160:163], v[14:17]\n"
      "v_and_b32 v111, 0xffff0000, v42\n"
      "s_waitcnt lgkmcnt(5)\n"
      "v_mfma_f32_16x16x32_f16 v[2:5], v[168:171], v[164:167], v[2:5]\n"
      "v_lshlrev_b32 v42, 16, v42\n"
      "s_add_u32 m0, s5, 0x24680\n"
      "s_nop 0\n"
      "global_load_lds_dwordx4 v81, s[6:7]\n"
      "ds_read_b64 v[164:165], v95 offset:1088\n"
      "ds_read_b64 v[166:167], v95 offset:1096\n"
      "v_mfma_f32_16x16x32_f16 v[6:9], v[168:171], v[176:179], v[6:9]\n"
      "v_add_f32 v111, v111, v90\n"
      "ds_read_b64 v[188:189], v95 offset:3584\n"
      "ds_read_b64 v[190:191], v95 offset:3592\n"
      "ds_read_b128 v[192:195], v94 offset:2560\n"
      "v_mfma_f32_16x16x32_f16 v[10:13], v[168:171], v[160:163], v[10:13]\n"
      "v_fma_mix_f32 v84, v55, v111, v42 op_sel:[0,0,0] op_sel_hi:[1,0,0]\n"
      "ds_read_b128 v[196:199], v94 offset:2048\n"
      "s_waitcnt lgkmcnt(10)\n"
      "v_mfma_f32_16x16x32_f16 v[14:17], v[168:171], v[172:175], v[14:17]\n"
      "v_and_b32 v111, 0xffff0000, v44\n"
      "ds_read_b128 v[168:171], v94 offset:1536\n"
      "s_waitcnt lgkmcnt(9)\n"
      "v_mfma_f32_16x16x32_f16 v[2:5], v[180:183], v[176:179], v[2:5]\n"
      "v_lshlrev_b32 v44, 16, v44\n"
      "ds_read_b128 v[176:179], v94 offset:1024\n"
      "v_mfma_f32_16x16x32_f16 v[6:9], v[180:183], v[160:163], v[6:9]\n"
      "v_add_f32 v111, v111, v90\n"
      "ds_read_b64 v[200:201], v95 offset:3072\n"
      "ds_read_b64 v[202:203], v95 offset:3080\n"
      "v_mfma_f32_16x16x32_f16 v[10:13], v[180:183], v[172:175], v[10:13]\n"
      "v_fma_mix_f32 v85, v55, v111, v44 op_sel:[1,0,0] op_sel_hi:[1,0,0]\n"
      "s_waitcnt lgkmcnt(10)\n"
      "v_mfma_f32_16x16x32_f16 v[2:5], v[184:187], v[160:163], v[2:5]\n"
      "v_mfma_f32_16x16x32_f16 v[6:9], v[184:187], v[172:175], v[6:9]\n"
      "v_and_b32 v111, 0xffff0000, v46\n"
      "ds_read_b128 v[160:163], v94 offset:512\n"
      "s_waitcnt lgkmcnt(9)\n"
      "v_mfma_f32_16x16x32_f16 v[2:5], v[164:167], v[172:175], v[2:5]\n"
      "v_lshlrev_b32 v46, 16, v46\n"
      "ds_read_b64 v[164:165], v95 offset:2560\n"
      "ds_read_b64 v[166:167], v95 offset:2568\n"
      "s_waitcnt lgkmcnt(8)\n"
      "v_mfma_f32_16x16x32_f16 v[2:5], v[188:191], v[192:195], v[2:5]\n"
      "v_add_f32 v111, v111, v90\n"
      "s_add_u32 m0, s5, 0x18680\n"
      "s_nop 0\n"
      "global_load_lds_dwordx4 v75, s[6:7]\n"
      "s_waitcnt lgkmcnt(7)\n"
      "v_mfma_f32_16x16x32_f16 v[6:9], v[188:191], v[196:199], v[6:9]\n"
      "v_fma_mix_f32 v86, v56, v111, v46 op_sel:[0,0,0] op_sel_hi:[1,0,0]\n"
      "s_waitcnt lgkmcnt(6)\n"
      "v_mfma_f32_16x16x32_f16 v[10:13], v[188:191], v[168:171], v[10:13]\n"
      "v_and_b32 v111, 0xffff0000, v48\n"
      "ds_read_b128 v[172:175], v94 offset:0\n"
      "s_waitcnt lgkmcnt(6)\n"
      "v_mfma_f32_16x16x32_f16 v[14:17], v[188:191], v[176:179], v[14:17]\n"
      "v_lshlrev_b32 v48, 16, v48\n"
      "s_add_u32 m0, s5, 0x1a680\n"
      "s_nop 0\n"
      "global_load_lds_dwordx4 v76, s[6:7]\n"
      "ds_read_b64 v[180:181], v95 offset:2048\n"
      "ds_read_b64 v[182:183], v95 offset:2056\n"
      "s_waitcnt lgkmcnt(6)\n"
      "v_mfma_f32_16x16x32_f16 v[2:5], v[200:203], v[196:199], v[2:5]\n"
      "v_add_f32 v111, v111, v90\n"
      "v_mfma_f32_16x16x32_f16 v[6:9], v[200:203], v[168:171], v[6:9]\n"
      "v_fma_mix_f32 v87, v56, v111, v48 op_sel:[1,0,0] op_sel_hi:[1,0,0]\n"
      "v_mfma_f32_16x16x32_f16 v[10:13], v[200:203], v[176:179], v[10:13]\n"
      "v_and_b32 v111, 0xffff0000, v50\n"
      "ds_read_b64 v[184:185], v95 offset:1536\n"
      "ds_read_b64 v[186:187], v95 offset:1544\n"
      "s_waitcnt lgkmcnt(7)\n"
      "v_mfma_f32_16x16x32_f16 v[14:17], v[200:203], v[160:163], v[14:17]\n"
      "v_lshlrev_b32 v50, 16, v50\n"
      "s_waitcnt lgkmcnt(5)\n"
      "v_mfma_f32_16x16x32_f16 v[2:5], v[164:167], v[168:171], v[2:5]\n"
      "v_add_f32 v111, v111, v90\n"
      "ds_read_b64 v[168:169], v95 offset:1024\n"
      "ds_read_b64 v[170:171], v95 offset:1032\n"
      "v_mfma_f32_16x16x32_f16 v[6:9], v[164:167], v[176:179], v[6:9]\n"
      "v_fma_mix_f32 v88, v57, v111, v50 op_sel:[0,0,0] op_sel_hi:[1,0,0]\n"
      "v_mfma_f32_16x16x32_f16 v[10:13], v[164:167], v[160:163], v[10:13]\n"
      "v_and_b32 v111, 0xffff0000, v52\n"
      "s_waitcnt lgkmcnt(6)\n"
      "v_mfma_f32_16x16x32_f16 v[14:17], v[164:167], v[172:175], v[14:17]\n"
      "v_lshlrev_b32 v52, 16, v52\n"
      "s_waitcnt lgkmcnt(4)\n"
      "v_mfma_f32_16x16x32_f16 v[2:5], v[180:183], v[176:179], v[2:5]\n"
      "v_add_f32 v111, v111, v90\n"
      "v_mfma_f32_16x16x32_f16 v[6:9], v[180:183], v[160:163], v[6:9]\n"
      "v_fma_mix_f32 v89, v57, v111, v52 op_sel:[1,0,0] op_sel_hi:[1,0,0]\n"
      "v_mfma_f32_16x16x32_f16 v[10:13], v[180:183], v[172:175], v[10:13]\n"
      "s_waitcnt lgkmcnt(2)\n"
      "v_mfma_f32_16x16x32_f16 v[2:5], v[184:187], v[160:163], v[2:5]\n"
      "v_mfma_f32_16x16x32_f16 v[6:9], v[184:187], v[172:175], v[6:9]\n"
      "s_waitcnt lgkmcnt(0)\n"
      "v_mfma_f32_16x16x32_f16 v[2:5], v[168:171], v[172:175], v[2:5]\n"
      "s_barrier\n"
      "s_nop 6\n"
      "ds_write_b128 v93, v[2:5] offset:6400\n"
      "ds_write_b128 v93, v[6:9] offset:5120\n"
      "ds_write_b128 v93, v[10:13] offset:3840\n"
      "ds_write_b128 v93, v[14:17] offset:2560\n"
      "L_end_4024:\n"
      "s_waitcnt lgkmcnt(0)\n"
      "s_barrier\n"
      "ds_read_b128 v[2:5], v98 offset:0\n"
      "ds_read_b128 v[6:9], v98 offset:20480\n"
      "ds_read_b128 v[10:13], v98 offset:16\n"
      "ds_read_b128 v[14:17], v98 offset:20496\n"
      "s_waitcnt lgkmcnt(2)\n"
      "v_add_f32 v2, v2, v6\n"
      "v_fmac_f32 v82, v2, v96\n"
      "v_add_f32 v3, v3, v7\n"
      "v_fmac_f32 v83, v3, v96\n"
      "v_add_f32 v4, v4, v8\n"
      "v_fmac_f32 v84, v4, v96\n"
      "v_add_f32 v5, v5, v9\n"
      "v_fmac_f32 v85, v5, v96\n"
      "s_waitcnt lgkmcnt(0)\n"
      "v_add_f32 v10, v10, v14\n"
      "v_fmac_f32 v86, v10, v96\n"
      "v_add_f32 v11, v11, v15\n"
      "v_fmac_f32 v87, v11, v96\n"
      "v_add_f32 v12, v12, v16\n"
      "v_fmac_f32 v88, v12, v96\n"
      "v_add_f32 v13, v13, v17\n"
      "v_fmac_f32 v89, v13, v96\n"
      "v_cvt_pk_bf16_f32 v2, v82, v83\n"
      "v_cvt_pk_bf16_f32 v3, v84, v85\n"
      "v_cvt_pk_bf16_f32 v4, v86, v87\n"
      "v_cvt_pk_bf16_f32 v5, v88, v89\n"
      "global_store_dwordx4 v97, v[2:5], s[48:49] sc1\n"
      "v_lshrrev_b32 v97, 4, v106\n"
      "v_and_b32 v98, 15, v106\n"
      "s_lshl_b32 s8, s33, 12\n"
      "s_add_u32 s43, s53, s8\n"
      "s_add_u32 s4, s18, s43\n"
      "s_addc_u32 s5, s19, 0\n"
      "s_add_u32 s30, s4, 0x8000\n"
      "s_addc_u32 s31, s5, 0\n"
      "s_add_u32 s44, s4, 0x10000\n"
      "s_addc_u32 s45, s5, 0\n"
      "s_add_u32 s48, s4, 0x18000\n"
      "s_addc_u32 s49, s5, 0\n"
      "v_and_b32 v111, 3, v106\n"
      "v_and_b32 v110, 1, v97\n"
      "v_cmp_eq_u32 s[22:23], v111, v110\n"
      "v_mov_b32 v109, s14\n"
      "v_mov_b32 v110, 0\n"
      "v_cndmask_b32 v50, v110, v109, s[22:23]\n"
      "v_cndmask_b32 v51, v110, v109, s[22:23]\n"
      "v_cndmask_b32 v52, v110, v109, s[22:23]\n"
      "v_cndmask_b32 v53, v110, v109, s[22:23]\n"
      "v_lshrrev_b32 v111, 5, v106\n"
      "v_bfe_u32 v110, v106, 2, 2\n"
      "v_lshl_add_u32 v103, v111, 3, v110\n"
      "v_add_u32 v110, v110, v103\n"
      "v_mul_u32_u24 v111, 0xe0, v110\n"
      "v_and_b32 v110, s73, v104\n"
      "v_lshl_add_u32 v111, v110, 1, v111\n"
      "v_and_b32 v109, 3, v106\n"
      "v_lshl_add_u32 v111, v109, 3, v111\n"
      "v_add_u32 v99, s60, v111\n"
      "v_add_u32 v100, s61, v111\n"
      "v_add_u32 v101, s62, v111\n"
      "v_add_u32 v102, s63, v111\n"
      "s_waitcnt vmcnt(1)\n"
      "s_barrier\n"
      "ds_read_b64_tr_b16 v[192:193], v99 offset:0\n"
      "ds_read_b64_tr_b16 v[194:195], v99 offset:224\n"
      "ds_read_b64_tr_b16 v[196:197], v99 offset:3584\n"
      "ds_read_b64_tr_b16 v[198:199], v99 offset:3808\n"
      "ds_read_b64_tr_b16 v[200:201], v99 offset:7168\n"
      "ds_read_b64_tr_b16 v[202:203], v99 offset:7392\n"
      "ds_read_b64_tr_b16 v[204:205], v99 offset:10752\n"
      "ds_read_b64_tr_b16 v[206:207], v99 offset:10976\n"
      "ds_read_b64_tr_b16 v[208:209], v100 offset:0\n"
      "ds_read_b64_tr_b16 v[210:211], v100 offset:224\n"
      "ds_read_b64_tr_b16 v[212:213], v100 offset:3584\n"
      "ds_read_b64_tr_b16 v[214:215], v100 offset:3808\n"
      "ds_read_b64_tr_b16 v[216:217], v100 offset:7168\n"
      "ds_read_b64_tr_b16 v[218:219], v100 offset:7392\n"
      "ds_read_b64_tr_b16 v[220:221], v100 offset:10752\n"
      "ds_read_b64_tr_b16 v[222:223], v100 offset:10976\n"
      "ds_read_b64_tr_b16 v[224:225], v101 offset:0\n"
      "ds_read_b64_tr_b16 v[226:227], v101 offset:224\n"
      "ds_read_b64_tr_b16 v[228:229], v101 offset:3584\n"
      "ds_read_b64_tr_b16 v[230:231], v101 offset:3808\n"
      "ds_read_b64_tr_b16 v[232:233], v101 offset:7168\n"
      "ds_read_b64_tr_b16 v[234:235], v101 offset:7392\n"
      "ds_read_b64_tr_b16 v[236:237], v101 offset:10752\n"
      "ds_read_b64_tr_b16 v[238:239], v101 offset:10976\n"
      "ds_read_b64_tr_b16 v[240:241], v102 offset:0\n"
      "ds_read_b64_tr_b16 v[242:243], v102 offset:224\n"
      "ds_read_b64_tr_b16 v[244:245], v102 offset:3584\n"
      "ds_read_b64_tr_b16 v[246:247], v102 offset:3808\n"
      "ds_read_b64_tr_b16 v[248:249], v102 offset:7168\n"
      "ds_read_b64_tr_b16 v[250:251], v102 offset:7392\n"
      "ds_read_b64_tr_b16 v[252:253], v102 offset:10752\n"
      "ds_read_b64_tr_b16 v[254:255], v102 offset:10976\n"
      "v_lshlrev_b32 v111, 6, v103\n"
      "v_bfe_u32 v110, v106, 4, 1\n"
      "v_lshl_add_u32 v111, v110, 5, v111\n"
      "v_and_b32 v110, 3, v106\n"
      "v_lshl_add_u32 v111, v110, 3, v111\n"
      "v_add_u32 v99, s64, v111\n"
      "v_add_u32 v100, s65, v111\n"
      "v_add_u32 v101, s66, v111\n"
      "v_add_u32 v102, s67, v111\n"
      "v_and_b32 v111, 31, v106\n"
      "v_mul_u32_u24 v111, 48, v111\n"
      "v_add_u32 v94, 0x26000, v111\n"
      "s_lshl_b32 s43, s33, 11\n"
      "v_add_u32 v95, s43, v108\n"
      "v_xor_b32 v96, 0x800, v95\n"
      "s_mov_b32 s50, 0xaaaaaaaa\n"
      "s_mov_b32 s51, 0xaaaaaaaa\n"
      "s_mov_b32 s52, 0xcccccccc\n"
      "s_mov_b32 s53, 0xcccccccc\n"
      "s_mov_b32 s58, 0xff00ff00\n"
      "s_mov_b32 s59, 0xff00ff00\n"
      "s_mov_b32 s60, 0xffff0000\n"
      "s_mov_b32 s61, 0xffff0000\n"
      "s_mov_b32 s62, 0xf0f0f0f\n"
      "s_mov_b32 s63, 0xf0f0f0f0\n"
      "v_lshl_add_u32 v58, v104, 2, s70\n"
      "v_lshl_add_u32 v109, v104, 2, s69\n"
      "v_add_u32 v111, s71, v104\n"
      "v_min_u32 v111, 0x6f, v111\n"
      "v_add_u32 v111, v111, v111\n"
      "v_add_u32 v105, 0x27dc0, v111\n"
      "s_cmp_eq_u32 s36, 3\n"
      "s_cbranch_scc0 L_skip_6030\n"
      "s_waitcnt lgkmcnt(0)\n"
      "s_mov_b32 exec_lo, s74\n"
      "s_mov_b32 exec_hi, s74\n"
      "v_mov_b32 v192, 0x3f803f80\n"
      "v_mov_b32 v193, 0x3f803f80\n"
      "v_mov_b32 v194, 0x3f803f80\n"
      "v_mov_b32 v195, 0x3f803f80\n"
      "v_mov_b32 v196, 0x3f803f80\n"
      "v_mov_b32 v197, 0x3f803f80\n"
      "v_mov_b32 v198, 0x3f803f80\n"
      "v_mov_b32 v199, 0x3f803f80\n"
      "v_mov_b32 v200, 0x3f803f80\n"
      "v_mov_b32 v201, 0x3f803f80\n"
      "v_mov_b32 v202, 0x3f803f80\n"
      "v_mov_b32 v203, 0x3f803f80\n"
      "v_mov_b32 v204, 0x3f803f80\n"
      "v_mov_b32 v205, 0x3f803f80\n"
      "v_mov_b32 v206, 0x3f803f80\n"
      "v_mov_b32 v207, 0x3f803f80\n"
      "v_mov_b32 v208, 0x3f803f80\n"
      "v_mov_b32 v209, 0x3f803f80\n"
      "v_mov_b32 v210, 0x3f803f80\n"
      "v_mov_b32 v211, 0x3f803f80\n"
      "v_mov_b32 v212, 0x3f803f80\n"
      "v_mov_b32 v213, 0x3f803f80\n"
      "v_mov_b32 v214, 0x3f803f80\n"
      "v_mov_b32 v215, 0x3f803f80\n"
      "v_mov_b32 v216, 0x3f803f80\n"
      "v_mov_b32 v217, 0x3f803f80\n"
      "v_mov_b32 v218, 0x3f803f80\n"
      "v_mov_b32 v219, 0x3f803f80\n"
      "v_mov_b32 v220, 0x3f803f80\n"
      "v_mov_b32 v221, 0x3f803f80\n"
      "v_mov_b32 v222, 0x3f803f80\n"
      "v_mov_b32 v223, 0x3f803f80\n"
      "v_mov_b32 v224, 0x3f803f80\n"
      "v_mov_b32 v225, 0x3f803f80\n"
      "v_mov_b32 v226, 0x3f803f80\n"
      "v_mov_b32 v227, 0x3f803f80\n"
      "v_mov_b32 v228, 0x3f803f80\n"
      "v_mov_b32 v229, 0x3f803f80\n"
      "v_mov_b32 v230, 0x3f803f80\n"
      "v_mov_b32 v231, 0x3f803f80\n"
      "v_mov_b32 v232, 0x3f803f80\n"
      "v_mov_b32 v233, 0x3f803f80\n"
      "v_mov_b32 v234, 0x3f803f80\n"
      "v_mov_b32 v235, 0x3f803f80\n"
      "v_mov_b32 v236, 0x3f803f80\n"
      "v_mov_b32 v237, 0x3f803f80\n"
      "v_mov_b32 v238, 0x3f803f80\n"
      "v_mov_b32 v239, 0x3f803f80\n"
      "v_mov_b32 v240, 0x3f803f80\n"
      "v_mov_b32 v241, 0x3f803f80\n"
      "v_mov_b32 v242, 0x3f803f80\n"
      "v_mov_b32 v243, 0x3f803f80\n"
      "v_mov_b32 v244, 0x3f803f80\n"
      "v_mov_b32 v245, 0x3f803f80\n"
      "v_mov_b32 v246, 0x3f803f80\n"
      "v_mov_b32 v247, 0x3f803f80\n"
      "v_mov_b32 v248, 0x3f803f80\n"
      "v_mov_b32 v249, 0x3f803f80\n"
      "v_mov_b32 v250, 0x3f803f80\n"
      "v_mov_b32 v251, 0x3f803f80\n"
      "v_mov_b32 v252, 0x3f803f80\n"
      "v_mov_b32 v253, 0x3f803f80\n"
      "v_mov_b32 v254, 0x3f803f80\n"
      "v_mov_b32 v255, 0x3f803f80\n"
      "s_mul_i32 s43, s74, 0xfffe\n"
      "s_mov_b32 exec_lo, s43\n"
      "s_mov_b32 exec_hi, s43\n"
      "v_mov_b32 v192, 0\n"
      "v_mov_b32 v193, 0\n"
      "v_mov_b32 v194, 0\n"
      "v_mov_b32 v195, 0\n"
      "v_mov_b32 v196, 0\n"
      "v_mov_b32 v197, 0\n"
      "v_mov_b32 v198, 0\n"
      "v_mov_b32 v199, 0\n"
      "v_mov_b32 v200, 0\n"
      "v_mov_b32 v201, 0\n"
      "v_mov_b32 v202, 0\n"
      "v_mov_b32 v203, 0\n"
      "v_mov_b32 v204, 0\n"
      "v_mov_b32 v205, 0\n"
      "v_mov_b32 v206, 0\n"
      "v_mov_b32 v207, 0\n"
      "v_mov_b32 v208, 0\n"
      "v_mov_b32 v209, 0\n"
      "v_mov_b32 v210, 0\n"
      "v_mov_b32 v211, 0\n"
      "v_mov_b32 v212, 0\n"
      "v_mov_b32 v213, 0\n"
      "v_mov_b32 v214, 0\n"
      "v_mov_b32 v215, 0\n"
      "v_mov_b32 v216, 0\n"
      "v_mov_b32 v217, 0\n"
      "v_mov_b32 v218, 0\n"
      "v_mov_b32 v219, 0\n"
      "v_mov_b32 v220, 0\n"
      "v_mov_b32 v221, 0\n"
      "v_mov_b32 v222, 0\n"
      "v_mov_b32 v223, 0\n"
      "v_mov_b32 v224, 0\n"
      "v_mov_b32 v225, 0\n"
      "v_mov_b32 v226, 0\n"
      "v_mov_b32 v227, 0\n"
      "v_mov_b32 v228, 0\n"
      "v_mov_b32 v229, 0\n"
      "v_mov_b32 v230, 0\n"
      "v_mov_b32 v231, 0\n"
      "v_mov_b32 v232, 0\n"
      "v_mov_b32 v233, 0\n"
      "v_mov_b32 v234, 0\n"
      "v_mov_b32 v235, 0\n"
      "v_mov_b32 v236, 0\n"
      "v_mov_b32 v237, 0\n"
      "v_mov_b32 v238, 0\n"
      "v_mov_b32 v239, 0\n"
      "v_mov_b32 v240, 0\n"
      "v_mov_b32 v241, 0\n"
      "v_mov_b32 v242, 0\n"
      "v_mov_b32 v243, 0\n"
      "v_mov_b32 v244, 0\n"
      "v_mov_b32 v245, 0\n"
      "v_mov_b32 v246, 0\n"
      "v_mov_b32 v247, 0\n"
      "v_mov_b32 v248, 0\n"
      "v_mov_b32 v249, 0\n"
      "v_mov_b32 v250, 0\n"
      "v_mov_b32 v251, 0\n"
      "v_mov_b32 v252, 0\n"
      "v_mov_b32 v253, 0\n"
      "v_mov_b32 v254, 0\n"
      "v_mov_b32 v255, 0\n"
      "s_mov_b64 exec, -1\n"
      "L_skip_6030:\n"
      "s_waitcnt lgkmcnt(15)\n"
      "v_mfma_f32_16x16x32_bf16 v[54:57], v[50:53], v[192:195], 0\n"
      "v_mfma_f32_16x16x32_bf16 v[54:57], v[50:53], v[196:199], v[54:57]\n"
      "v_mfma_f32_16x16x32_bf16 v[54:57], v[50:53], v[200:203], v[54:57]\n"
      "v_mfma_f32_16x16x32_bf16 v[54:57], v[50:53], v[204:207], v[54:57]\n"
      "v_mfma_f32_16x16x32_bf16 v[54:57], v[50:53], v[208:211], v[54:57]\n"
      "v_mfma_f32_16x16x32_bf16 v[54:57], v[50:53], v[212:215], v[54:57]\n"
      "v_mfma_f32_16x16x32_bf16 v[54:57], v[50:53], v[216:219], v[54:57]\n"
      "v_mfma_f32_16x16x32_bf16 v[54:57], v[50:53], v[220:223], v[54:57]\n"
      "s_waitcnt lgkmcnt(14)\n"
      "v_mfma_f32_16x16x32_bf16 v[54:57], v[50:53], v[224:227], v[54:57]\n"
      "s_waitcnt lgkmcnt(12)\n"
      "v_mfma_f32_16x16x32_bf16 v[54:57], v[50:53], v[228:231], v[54:57]\n"
      "s_waitcnt lgkmcnt(10)\n"
      "v_mfma_f32_16x16x32_bf16 v[54:57], v[50:53], v[232:235], v[54:57]\n"
      "s_waitcnt lgkmcnt(8)\n"
      "v_mfma_f32_16x16x32_bf16 v[54:57], v[50:53], v[236:239], v[54:57]\n"
      "s_waitcnt lgkmcnt(6)\n"
      "v_mfma_f32_16x16x32_bf16 v[54:57], v[50:53], v[240:243], v[54:57]\n"
      "s_waitcnt lgkmcnt(4)\n"
      "v_mfma_f32_16x16x32_bf16 v[54:57], v[50:53], v[244:247], v[54:57]\n"
      "s_waitcnt lgkmcnt(2)\n"
      "v_mfma_f32_16x16x32_bf16 v[54:57], v[50:53], v[248:251], v[54:57]\n"
      "s_waitcnt lgkmcnt(0)\n"
      "v_mfma_f32_16x16x32_bf16 v[54:57], v[50:53], v[252:255], v[54:57]\n"
      "s_nop 7\n"
      "v_cndmask_b32 v54, v54, v55, s[60:61]\n"
      "ds_write_b32 v109, v54\n"
      "s_waitcnt lgkmcnt(0)\n"
      "s_barrier\n"
      "s_add_u32 m0, s8, 0xe000\n"
      "s_nop 0\n"
      "global_load_lds_dwordx4 v108, s[4:5] offset:0 sc1\n"
      "global_load_lds_dwordx4 v108, s[4:5] offset:1024 sc1\n"
      "global_load_lds_dwordx4 v108, s[4:5] offset:2048 sc1\n"
      "global_load_lds_dwordx4 v108, s[4:5] offset:3072 sc1\n"
      "s_or_b32 s43, s2, s33\n"
      "s_cbranch_scc1 L_skip_6031\n"
      "v_mov_b32 v111, 0\n"
      "v_mov_b32 v110, s38\n"
      "s_mov_b64 exec, 1\n"
      "global_store_dword v111, v110, s[18:19] sc1\n"
      "s_mov_b64 exec, -1\n"
      "L_skip_6031:\n"
      "ds_read_b32 v55, v58\n"
      "ds_read_u16 v105, v105\n"
      "v_mov_b32 v34, 0\n"
      "v_mov_b32 v35, 0\n"
      "v_mov_b32 v36, 0\n"
      "v_mov_b32 v37, 0\n"
      "v_mov_b32 v40, 0\n"
      "v_mov_b32 v41, 0\n"
      "s_waitcnt lgkmcnt(1)\n"
      "v_add_f32 v54, v54, v55\n"
      "v_cvt_pk_bf16_f32 v111, v54, 0\n"
      "v_lshlrev_b32 v110, 16, v111\n"
      "v_sub_f32 v110, v54, v110\n"
      "v_cvt_pk_bf16_f32 v110, v110, 0\n"
      "s_mov_b32 exec_hi, 0\n"
      "v_lshl_or_b32 v34, v110, 16, v111\n"
      "s_waitcnt lgkmcnt(0)\n"
      "v_mov_b32 v35, v105\n"
      "s_mov_b64 exec, -1\n"
      "s_add_u32 s70, s18, s54\n"
      "s_addc_u32 s71, s19, 0\n"
      "v_lshrrev_b32 v111, 1, v97\n"
      "v_lshlrev_b32 v111, 10, v111\n"
      "v_and_b32 v110, 1, v97\n"
      "v_lshl_add_u32 v111, v110, 9, v111\n"
      "v_lshl_add_u32 v111, v98, 4, v111\n"
      "v_add_u32 v1, s72, v111\n"
      "v_add_u32 v103, 0x2000, v1\n"
      "v_add_u32 v97, s68, v94\n"
      "v_mov_b32 v98, 0x3727c5ac\n"
      "s_waitcnt vmcnt(0)\n"
      "L_hb_6032:\n"
      "s_add_u32 s43, s8, 0xe000\n"
      "v_add_u32 v109, s43, v108\n"
      "ds_read_b128 v[42:45], v109 offset:0\n"
      "ds_read_b128 v[46:49], v109 offset:1024\n"
      "s_waitcnt lgkmcnt(1)\n"
      "v_max3_u32 v111, v42, v43, v44\n"
      "s_waitcnt lgkmcnt(0)\n"
      "v_max3_u32 v110, v46, v47, v48\n"
      "v_max3_u32 v111, v111, v45, v49\n"
      "ds_read_b128 v[42:45], v109 offset:2048\n"
      "ds_read_b128 v[46:49], v109 offset:3072\n"
      "s_waitcnt lgkmcnt(1)\n"
      "v_max3_u32 v111, v111, v42, v43\n"
      "v_max3_u32 v110, v110, v44, v45\n"
      "s_waitcnt lgkmcnt(0)\n"
      "v_max3_u32 v111, v111, v46, v47\n"
      "v_max3_u32 v110, v110, v48, v49\n"
      "v_max_u32 v111, v111, v110\n"
      "v_cmp_eq_u32 vcc, -1, v111\n"
      "s_nop 0\n"
      "s_cbranch_vccnz L_slow_6034\n"
      "L_ha_6033:\n"
      "s_add_u32 m0, s8, 0x16000\n"
      "s_nop 0\n"
      "global_load_lds_dwordx4 v108, s[30:31] offset:0 sc1\n"
      "global_load_lds_dwordx4 v108, s[30:31] offset:1024 sc1\n"
      "global_load_lds_dwordx4 v108, s[30:31] offset:2048 sc1\n"
      "global_load_lds_dwordx4 v108, s[30:31] offset:3072 sc1\n"
      "s_barrier\n"
      "s_add_u32 s10, s12, 0x5000\n"
      "s_addc_u32 s11, s13, 0\n"
      "s_add_u32 s28, s10, 0x1000\n"
      "s_addc_u32 s29, s11, 0\n"
      "s_mov_b32 s6, 0\n"
      "L_gemm1:\n"
      "s_cmp_eq_u32 s42, 0\n"
      "s_cbranch_scc0 L_skip_8035\n"
      "ds_read_b64_tr_b16 v[82:83], v99 offset:0\n"
      "ds_read_b64_tr_b16 v[84:85], v99 offset:256\n"
      "ds_read_b64_tr_b16 v[86:87], v99 offset:1024\n"
      "ds_read_b64_tr_b16 v[88:89], v99 offset:1280\n"
      "s_waitcnt lgkmcnt(2)\n"
      "v_mfma_f32_32x32x16_bf16 v[2:17], v[192:195], v[82:85], 0\n"
      "ds_read_b64_tr_b16 v[90:91], v99 offset:2048\n"
      "ds_read_b64_tr_b16 v[92:93], v99 offset:2304\n"
      "s_add_u32 m0, s8, 0x1e000\n"
      "s_nop 0\n"
      "global_load_lds_dwordx4 v108, s[44:45] offset:0 sc1\n"
      "s_waitcnt lgkmcnt(2)\n"
      "v_mfma_f32_32x32x16_bf16 v[2:17], v[196:199], v[86:89], v[2:17]\n"
      "ds_read_b64_tr_b16 v[82:83], v99 offset:3072\n"
      "ds_read_b64_tr_b16 v[84:85], v99 offset:3328\n"
      "v_add_u32 v99, 0x8000, v99\n"
      "global_load_lds_dwordx4 v108, s[44:45] offset:1024 sc1\n"
      "s_waitcnt lgkmcnt(2)\n"
      "v_mfma_f32_32x32x16_bf16 v[2:17], v[200:203], v[90:93], v[2:17]\n"
      "ds_read_b64_tr_b16 v[86:87], v100 offset:0\n"
      "ds_read_b64_tr_b16 v[88:89], v100 offset:256\n"
      "s_waitcnt lgkmcnt(2)\n"
      "v_mfma_f32_32x32x16_bf16 v[2:17], v[204:207], v[82:85], v[2:17]\n"
      "ds_read_b64_tr_b16 v[90:91], v100 offset:1024\n"
      "ds_read_b64_tr_b16 v[92:93], v100 offset:1280\n"
      "global_load_lds_dwordx4 v108, s[44:45] offset:2048 sc1\n"
      "s_waitcnt lgkmcnt(2)\n"
      "v_mfma_f32_32x32x16_bf16 v[2:17], v[208:211], v[86:89], v[2:17]\n"
      "global_load_lds_dwordx4 v108, s[44:45] offset:3072 sc1\n"
      "v_mfma_f32_32x32x16_bf16 v[50:65], v[86:89], v[86:89], 0\n"
      "ds_read_b64_tr_b16 v[82:83], v100 offset:2048\n"
      "ds_read_b64_tr_b16 v[84:85], v100 offset:2304\n"
      "s_waitcnt lgkmcnt(2)\n"
      "v_mfma_f32_32x32x16_bf16 v[2:17], v[212:215], v[90:93], v[2:17]\n"
      "s_add_u32 m0, s8, 0\n"
      "s_nop 0\n"
      "global_load_lds_dwordx4 v108, s[48:49] offset:0 sc1\n"
      "v_mfma_f32_32x32x16_bf16 v[50:65], v[90:93], v[90:93], v[50:65]\n"
      "ds_read_b64_tr_b16 v[86:87], v100 offset:3072\n"
      "ds_read_b64_tr_b16 v[88:89], v100 offset:3328\n"
      "v_add_u32 v100, 0x8000, v100\n"
      "s_waitcnt lgkmcnt(2)\n"
      "v_mfma_f32_32x32x16_bf16 v[2:17], v[216:219], v[82:85], v[2:17]\n"
      "global_load_lds_dwordx4 v108, s[48:49] offset:1024 sc1\n"
      "v_mfma_f32_32x32x16_bf16 v[50:65], v[82:85], v[82:85], v[50:65]\n"
      "ds_read_b64_tr_b16 v[90:91], v101 offset:0\n"
      "ds_read_b64_tr_b16 v[92:93], v101 offset:256\n"
      "global_load_lds_dwordx4 v108, s[48:49] offset:2048 sc1\n"
      "s_waitcnt lgkmcnt(2)\n"
      "v_mfma_f32_32x32x16_bf16 v[2:17], v[220:223], v[86:89], v[2:17]\n"
      "v_mfma_f32_32x32x16_bf16 v[50:65], v[86:89], v[86:89], v[50:65]\n"
      "ds_read_b64_tr_b16 v[82:83], v101 offset:1024\n"
      "ds_read_b64_tr_b16 v[84:85], v101 offset:1280\n"
      "global_load_lds_dwordx4 v108, s[48:49] offset:3072 sc1\n"
      "s_waitcnt lgkmcnt(2)\n"
      "v_mfma_f32_32x32x16_bf16 v[2:17], v[224:227], v[90:93], v[2:17]\n"
      "s_cmp_eq_u32 s36, 0\n"
      "s_cbranch_scc0 L_skip_8036\n"
      "s_lshl_b32 s43, s37, 5\n"
      "v_bfe_u32 v111, v0, 1, 5\n"
      "v_add_u32 v111, s43, v111\n"
      "v_and_b32 v110, 1, v0\n"
      "s_add_u32 s43, s56, 0xe000\n"
      "v_lshlrev_b32 v109, 6, v111\n"
      "v_lshl_add_u32 v109, v110, 4, v109\n"
      "v_add_u32 v109, s43, v109\n"
      "ds_read_b128 v[46:49], v109\n"
      "v_lshlrev_b32 v110, 10, v110\n"
      "v_lshl_add_u32 v111, v111, 1, v110\n"
      "s_waitcnt lgkmcnt(0)\n"
      "ds_write_b16 v111, v46 offset:49152\n"
      "ds_write_b16_d16_hi v111, v46 offset:49280\n"
      "ds_write_b16 v111, v47 offset:49408\n"
      "ds_write_b16_d16_hi v111, v47 offset:49536\n"
      "ds_write_b16 v111, v48 offset:49664\n"
      "ds_write_b16_d16_hi v111, v48 offset:49792\n"
      "ds_write_b16 v111, v49 offset:49920\n"
      "ds_write_b16_d16_hi v111, v49 offset:50048\n"
      "L_skip_8036:\n"
      "v_mfma_f32_32x32x16_bf16 v[50:65], v[90:93], v[90:93], v[50:65]\n"
      "ds_read_b64_tr_b16 v[86:87], v101 offset:2048\n"
      "ds_read_b64_tr_b16 v[88:89], v101 offset:2304\n"
      "s_waitcnt lgkmcnt(2)\n"
      "v_mfma_f32_32x32x16_bf16 v[2:17], v[228:231], v[82:85], v[2:17]\n"
      "s_waitcnt vmcnt(8)\n"
      "s_barrier\n"
      "v_mfma_f32_32x32x16_bf16 v[50:65], v[82:85], v[82:85], v[50:65]\n"
      "ds_read_b64_tr_b16 v[90:91], v101 offset:3072\n"
      "ds_read_b64_tr_b16 v[92:93], v101 offset:3328\n"
      "v_add_u32 v101, 0x8000, v101\n"
      "s_waitcnt lgkmcnt(2)\n"
      "v_mfma_f32_32x32x16_bf16 v[2:17], v[232:235], v[86:89], v[2:17]\n"
      "global_load_dwordx4 v[112:115], v108, s[12:13] offset:0 sc1\n"
      "v_mfma_f32_32x32x16_bf16 v[50:65], v[86:89], v[86:89], v[50:65]\n"
      "ds_read_b64_tr_b16 v[82:83], v102 offset:0\n"
      "ds_read_b64_tr_b16 v[84:85], v102 offset:256\n"
      "s_waitcnt lgkmcnt(2)\n"
      "v_mfma_f32_32x32x16_bf16 v[2:17], v[236:239], v[90:93], v[2:17]\n"
      "global_load_dwordx4 v[116:119], v108, s[12:13] offset:1024 sc1\n"
      "v_mfma_f32_32x32x16_bf16 v[50:65], v[90:93], v[90:93], v[50:65]\n"
      "ds_read_b64_tr_b16 v[86:87], v102 offset:1024\n"
      "ds_read_b64_tr_b16 v[88:89], v102 offset:1280\n"
      "s_waitcnt lgkmcnt(2)\n"
      "v_mfma_f32_32x32x16_bf16 v[2:17], v[240:243], v[82:85], v[2:17]\n"
      "ds_read_b64_tr_b16 v[90:91], v102 offset:2048\n"
      "ds_read_b64_tr_b16 v[92:93], v102 offset:2304\n"
      "global_load_dwordx4 v[120:123], v108, s[12:13] offset:2048 sc1\n"
      "s_waitcnt lgkmcnt(2)\n"
      "v_mfma_f32_32x32x16_bf16 v[2:17], v[244:247], v[86:89], v[2:17]\n"
      "ds_read_b64_tr_b16 v[82:83], v102 offset:3072\n"
      "ds_read_b64_tr_b16 v[84:85], v102 offset:3328\n"
      "v_add_u32 v102, 0x8000, v102\n"
      "v_cndmask_b32 v50, v50, v51, s[50:51]\n"
      "v_cndmask_b32 v52, v52, v53, s[50:51]\n"
      "v_cndmask_b32 v54, v54, v55, s[50:51]\n"
      "v_cndmask_b32 v56, v56, v57, s[50:51]\n"
      "v_cndmask_b32 v58, v58, v59, s[50:51]\n"
      "v_cndmask_b32 v60, v60, v61, s[50:51]\n"
      "s_waitcnt lgkmcnt(2)\n"
      "v_mfma_f32_32x32x16_bf16 v[2:17], v[248:251], v[90:93], v[2:17]\n"
      "ds_read_b64_tr_b16 v[86:87], v99 offset:0\n"
      "ds_read_b64_tr_b16 v[88:89], v99 offset:256\n"
      "v_cndmask_b32 v62, v62, v63, s[50:51]\n"
      "v_cndmask_b32 v64, v64, v65, s[50:51]\n"
      "v_cndmask_b32 v50, v50, v52, s[52:53]\n"
      "v_cndmask_b32 v54, v54, v56, s[52:53]\n"
      "v_cndmask_b32 v58, v58, v60, s[52:53]\n"
      "global_load_dwordx4 v[124:127], v108, s[12:13] offset:3072 sc1\n"
      "s_add_u32 s12, s12, 0x1000\n"
      "s_addc_u32 s13, s13, 0\n"
      "s_waitcnt lgkmcnt(2)\n"
      "v_mfma_f32_32x32x16_bf16 v[2:17], v[252:255], v[82:85], v[2:17]\n"
      "ds_read_b64_tr_b16 v[90:91], v99 offset:1024\n"
      "ds_read_b64_tr_b16 v[92:93], v99 offset:1280\n"
      "v_cndmask_b32 v62, v62, v64, s[52:53]\n"
      "v_cndmask_b32 v50, v50, v54, s[58:59]\n"
      "v_cndmask_b32 v58, v58, v62, s[58:59]\n"
      "v_cndmask_b32 v50, v50, v58, s[60:61]\n"
      "s_mov_b64 exec, s[62:63]\n"
      "ds_write_b32 v97, v50 offset:0\n"
      "s_mov_b64 exec, -1\n"
      "s_waitcnt lgkmcnt(3)\n"
      "v_mfma_f32_32x32x16_bf16 v[18:33], v[192:195], v[86:89], 0\n"
      "ds_read_b64_tr_b16 v[82:83], v99 offset:2048\n"
      "ds_read_b64_tr_b16 v[84:85], v99 offset:2304\n"
      "global_load_dwordx4 v[128:131], v108, s[12:13] offset:0 sc1\n"
      "s_waitcnt lgkmcnt(3)\n"
      "v_mfma_f32_32x32x16_bf16 v[18:33], v[196:199], v[90:93], v[18:33]\n"
      "ds_read_b64_tr_b16 v[86:87], v99 offset:3072\n"
      "ds_read_b64_tr_b16 v[88:89], v99 offset:3328\n"
      "v_add_u32 v99, 0x8000, v99\n"
      "ds_write_b128 v95, v[10:13] offset:32768\n"
      "s_waitcnt lgkmcnt(3)\n"
      "v_mfma_f32_32x32x16_bf16 v[18:33], v[200:203], v[82:85], v[18:33]\n"
      "ds_read_b64_tr_b16 v[90:91], v100 offset:0\n"
      "ds_read_b64_tr_b16 v[92:93], v100 offset:256\n"
      "ds_write_b128 v95, v[14:17] offset:33792\n"
      "global_load_dwordx4 v[132:135], v108, s[12:13] offset:1024 sc1\n"
      "s_waitcnt lgkmcnt(4)\n"
      "v_mfma_f32_32x32x16_bf16 v[18:33], v[204:207], v[86:89], v[18:33]\n"
      "ds_read_b64_tr_b16 v[82:83], v100 offset:1024\n"
      "ds_read_b64_tr_b16 v[84:85], v100 offset:1280\n"
      "s_waitcnt lgkmcnt(3)\n"
      "v_mfma_f32_32x32x16_bf16 v[18:33], v[208:211], v[90:93], v[18:33]\n"
      "ds_read_b64_tr_b16 v[86:87], v100 offset:2048\n"
      "ds_read_b64_tr_b16 v[88:89], v100 offset:2304\n"
      "s_waitcnt vmcnt(10)\n"
      "s_waitcnt lgkmcnt(4)\n"
      "s_barrier\n"
      "ds_read_b128 v[42:45], v94 offset:0\n"
      "ds_read_b64 v[46:47], v94 offset:32\n"
      "ds_read_b128 v[66:69], v96 offset:32768\n"
      "ds_read_b128 v[70:73], v96 offset:33792\n"
      "global_load_dwordx4 v[136:139], v108, s[12:13] offset:2048 sc1\n"
      "s_waitcnt lgkmcnt(6)\n"
      "v_mfma_f32_32x32x16_bf16 v[18:33], v[212:215], v[82:85], v[18:33]\n"
      "ds_read_b64_tr_b16 v[90:91], v100 offset:3072\n"
      "ds_read_b64_tr_b16 v[92:93], v100 offset:3328\n"
      "v_add_u32 v100, 0x8000, v100\n"
      "s_waitcnt lgkmcnt(5)\n"
      "v_add_f32 v42, v42, v43\n"
      "v_add_f32 v44, v44, v45\n"
      "s_waitcnt lgkmcnt(4)\n"
      "v_add_f32 v46, v46, v47\n"
      "v_add_f32 v42, v42, v44\n"
      "v_mul_f32 v46, s15, v46\n"
      "v_fma_f32 v42, v42, s15, v98\n"
      "s_waitcnt lgkmcnt(3)\n"
      "v_add_f32 v66, v2, v66\n"
      "v_fma_f32 v42, -v46, v46, v42\n"
      "s_lshr_b32 s43, s8, 1\n"
      "s_add_u32 m0, s43, 0x11800\n"
      "s_nop 0\n"
      "global_load_lds_dwordx4 v108, s[28:29] offset:2048 sc1\n"
      "v_mfma_f32_32x32x16_bf16 v[18:33], v[216:219], v[86:89], v[18:33]\n"
      "ds_read_b64_tr_b16 v[82:83], v101 offset:0\n"
      "ds_read_b64_tr_b16 v[84:85], v101 offset:256\n"
      "v_add_f32 v67, v3, v67\n"
      "v_max_f32 v42, v42, v98\n"
      "v_add_f32 v68, v4, v68\n"
      "v_sqrt_f32 v42, v42\n"
      "v_add_f32 v69, v5, v69\n"
      "v_cvt_pk_bf16_f32 v38, -v46, -v46\n"
      "s_waitcnt lgkmcnt(4)\n"
      "v_add_f32 v70, v6, v70\n"
      "v_cvt_pk_bf16_f32 v39, v42, 0\n"
      "global_load_dwordx4 v[140:143], v108, s[12:13] offset:3072 sc1\n"
      "s_add_u32 s12, s12, 0x1000\n"
      "s_addc_u32 s13, s13, 0\n"
      "s_waitcnt lgkmcnt(2)\n"
      "v_mfma_f32_32x32x16_bf16 v[18:33], v[220:223], v[90:93], v[18:33]\n"
      "ds_read_b64_tr_b16 v[86:87], v101 offset:1024\n"
      "ds_read_b64_tr_b16 v[88:89], v101 offset:1280\n"
      "v_add_f32 v71, v7, v71\n"
      "v_lshlrev_b32 v42, 16, v39\n"
      "v_add_f32 v72, v8, v72\n"
      "v_rcp_f32 v106, v42\n"
      "v_add_f32 v73, v9, v73\n"
      "v_mul_f32 v105, 0x3fb8aa3b, v106\n"
      "s_nop 0\n"
      "v_mfma_f32_32x32x16_bf16 v[66:81], v[34:37], v[38:41], v[66:81]\n"
      "v_mul_f32 v104, -0.5, v106\n"
      "s_lshr_b32 s43, s8, 1\n"
      "s_add_u32 m0, s43, 0x11800\n"
      "s_nop 0\n"
      "global_load_lds_dwordx4 v108, s[28:29] offset:3072 sc1\n"
      "s_waitcnt lgkmcnt(2)\n"
      "v_mfma_f32_32x32x16_bf16 v[18:33], v[224:227], v[82:85], v[18:33]\n"
      "ds_read_b64_tr_b16 v[90:91], v101 offset:2048\n"
      "ds_read_b64_tr_b16 v[92:93], v101 offset:2304\n"
      "global_load_dwordx4 v[144:147], v108, s[12:13] offset:0 sc1\n"
      "s_cmp_eq_u32 s36, 1\n"
      "s_cbranch_scc0 L_skip_8037\n"
      "s_lshl_b32 s43, s37, 5\n"
      "v_bfe_u32 v111, v0, 1, 5\n"
      "v_add_u32 v111, s43, v111\n"
      "v_and_b32 v110, 1, v0\n"
      "s_add_u32 s43, s56, 0x16000\n"
      "v_lshlrev_b32 v109, 6, v111\n"
      "v_lshl_add_u32 v109, v110, 4, v109\n"
      "v_add_u32 v109, s43, v109\n"
      "ds_read_b128 v[46:49], v109\n"
      "v_lshlrev_b32 v110, 10, v110\n"
      "v_lshl_add_u32 v111, v111, 1, v110\n"
      "s_waitcnt lgkmcnt(0)\n"
      "ds_write_b16 v111, v46 offset:51200\n"
      "ds_write_b16_d16_hi v111, v46 offset:51328\n"
      "ds_write_b16 v111, v47 offset:51456\n"
      "ds_write_b16_d16_hi v111, v47 offset:51584\n"
      "ds_write_b16 v111, v48 offset:51712\n"
      "ds_write_b16_d16_hi v111, v48 offset:51840\n"
      "ds_write_b16 v111, v49 offset:51968\n"
      "ds_write_b16_d16_hi v111, v49 offset:52096\n"
      "L_skip_8037:\n"
      "s_waitcnt lgkmcnt(2)\n"
      "v_mfma_f32_32x32x16_bf16 v[18:33], v[228:231], v[86:89], v[18:33]\n"
      "ds_read_b64_tr_b16 v[82:83], v101 offset:3072\n"
      "ds_read_b64_tr_b16 v[84:85], v101 offset:3328\n"
      "v_add_u32 v101, 0x8000, v101\n"
      "v_fma_f32 v74, v66, v105, -1.0\n"
      "v_exp_f32 v74, v74\n"
      "v_fma_f32 v75, v67, v105, -1.0\n"
      "v_fmac_f32 v74, v74, v74\n"
      "v_exp_f32 v75, v75\n"
      "v_fma_f32 v76, v68, v105, -1.0\n"
      "v_add_f32 v74, 0.5, v74\n"
      "s_waitcnt lgkmcnt(2)\n"
      "v_mfma_f32_32x32x16_bf16 v[18:33], v[232:235], v[90:93], v[18:33]\n"
      "ds_read_b64_tr_b16 v[86:87], v102 offset:0\n"
      "ds_read_b64_tr_b16 v[88:89], v102 offset:256\n"
      "v_fmac_f32 v75, v75, v75\n"
      "v_exp_f32 v76, v76\n"
      "v_fma_f32 v77, v69, v105, -1.0\n"
      "v_rcp_f32 v74, v74\n"
      "v_add_f32 v75, 0.5, v75\n"
      "v_fmac_f32 v76, v76, v76\n"
      "v_exp_f32 v77, v77\n"
      "global_load_dwordx4 v[148:151], v108, s[12:13] offset:1024 sc1\n"
      "s_waitcnt lgkmcnt(2)\n"
      "v_mfma_f32_32x32x16_bf16 v[18:33], v[236:239], v[82:85], v[18:33]\n"
      "ds_read_b64_tr_b16 v[90:91], v102 offset:1024\n"
      "ds_read_b64_tr_b16 v[92:93], v102 offset:1280\n"
      "v_fma_f32 v74, v74, v104, v106\n"
      "v_rcp_f32 v75, v75\n"
      "v_add_f32 v76, 0.5, v76\n"
      "v_fmac_f32 v77, v77, v77\n"
      "v_mul_f32 v66, v66, v74\n"
      "v_fma_f32 v75, v75, v104, v106\n"
      "v_rcp_f32 v76, v76\n"
      "s_waitcnt lgkmcnt(2)\n"
      "v_mfma_f32_32x32x16_bf16 v[18:33], v[240:243], v[86:89], v[18:33]\n"
      "ds_read_b64_tr_b16 v[82:83], v102 offset:2048\n"
      "ds_read_b64_tr_b16 v[84:85], v102 offset:2304\n"
      "v_add_f32 v77, 0.5, v77\n"
      "v_mul_f32 v67, v67, v75\n"
      "v_fma_f32 v76, v76, v104, v106\n"
      "v_rcp_f32 v77, v77\n"
      "v_mul_f32 v68, v68, v76\n"
      "v_fma_f32 v77, v77, v104, v106\n"
      "v_mul_f32 v69, v69, v77\n"
      "global_load_dwordx4 v[152:155], v108, s[12:13] offset:2048 sc1\n"
      "s_waitcnt lgkmcnt(2)\n"
      "v_mfma_f32_32x32x16_bf16 v[18:33], v[244:247], v[90:93], v[18:33]\n"
      "ds_read_b64_tr_b16 v[86:87], v102 offset:3072\n"
      "ds_read_b64_tr_b16 v[88:89], v102 offset:3328\n"
      "v_add_u32 v102, 0x8000, v102\n"
      "v_cvt_pk_bf16_f32 v74, v66, v67\n"
      "v_cvt_pk_bf16_f32 v75, v68, v69\n"
      "v_fma_f32 v78, v70, v105, -1.0\n"
      "v_exp_f32 v78, v78\n"
      "v_fma_f32 v79, v71, v105, -1.0\n"
      "v_fmac_f32 v78, v78, v78\n"
      "v_exp_f32 v79, v79\n"
      "s_waitcnt lgkmcnt(2)\n"
      "v_mfma_f32_32x32x16_bf16 v[18:33], v[248:251], v[82:85], v[18:33]\n"
      "ds_read_b64_tr_b16 v[90:91], v99 offset:0\n"
      "ds_read_b64_tr_b16 v[92:93], v99 offset:256\n"
      "v_fma_f32 v80, v72, v105, -1.0\n"
      "v_add_f32 v78, 0.5, v78\n"
      "v_fmac_f32 v79, v79, v79\n"
      "v_exp_f32 v80, v80\n"
      "v_fma_f32 v81, v73, v105, -1.0\n"
      "v_rcp_f32 v78, v78\n"
      "v_add_f32 v79, 0.5, v79\n"
      "global_load_dwordx4 v[156:159], v108, s[12:13] offset:3072 sc1\n"
      "s_add_u32 s12, s12, 0x1000\n"
      "s_addc_u32 s13, s13, 0\n"
      "s_waitcnt lgkmcnt(2)\n"
      "v_mfma_f32_32x32x16_bf16 v[18:33], v[252:255], v[86:89], v[18:33]\n"
      "ds_read_b64_tr_b16 v[82:83], v99 offset:1024\n"
      "ds_read_b64_tr_b16 v[84:85], v99 offset:1280\n"
      "v_fmac_f32 v80, v80, v80\n"
      "v_exp_f32 v81, v81\n"
      "v_fma_f32 v78, v78, v104, v106\n"
      "v_rcp_f32 v79, v79\n"
      "v_add_f32 v80, 0.5, v80\n"
      "v_fmac_f32 v81, v81, v81\n"
      "s_waitcnt lgkmcnt(2)\n"
      "v_mfma_f32_32x32x16_bf16 v[2:17], v[192:195], v[90:93], 0\n"
      "ds_read_b64_tr_b16 v[86:87], v99 offset:2048\n"
      "ds_read_b64_tr_b16 v[88:89], v99 offset:2304\n"
      "s_waitcnt vmcnt(14)\n"
      "L_hb_8038:\n"
      "s_add_u32 s43, s8, 0\n"
      "v_add_u32 v109, s43, v108\n"
      "ds_read_b128 v[42:45], v109 offset:0\n"
      "ds_read_b128 v[46:49], v109 offset:1024\n"
      "v_mul_f32 v70, v70, v78\n"
      "v_fma_f32 v79, v79, v104, v106\n"
      "v_rcp_f32 v80, v80\n"
      "global_load_dwordx4 v[160:163], v108, s[12:13] offset:0 sc1\n"
      "s_waitcnt lgkmcnt(4)\n"
      "v_mfma_f32_32x32x16_bf16 v[2:17], v[196:199], v[82:85], v[2:17]\n"
      "ds_read_b64_tr_b16 v[90:91], v99 offset:3072\n"
      "ds_read_b64_tr_b16 v[92:93], v99 offset:3328\n"
      "v_add_u32 v99, 0xfffe2000, v99\n"
      "s_waitcnt lgkmcnt(3)\n"
      "v_max3_u32 v111, v42, v43, v44\n"
      "s_waitcnt lgkmcnt(2)\n"
      "v_max3_u32 v110, v46, v47, v48\n"
      "v_max3_u32 v111, v111, v45, v49\n"
      "ds_read_b128 v[42:45], v109 offset:2048\n"
      "ds_read_b128 v[46:49], v109 offset:3072\n"
      "v_add_f32 v81, 0.5, v81\n"
      "v_mul_f32 v71, v71, v79\n"
      "v_fma_f32 v80, v80, v104, v106\n"
      "ds_write_b128 v95, v[26:29] offset:57344\n"
      "global_load_dwordx4 v[164:167], v108, s[12:13] offset:1024 sc1\n"
      "v_mfma_f32_32x32x16_bf16 v[2:17], v[200:203], v[86:89], v[2:17]\n"
      "ds_read_b64_tr_b16 v[82:83], v100 offset:0\n"
      "ds_read_b64_tr_b16 v[84:85], v100 offset:256\n"
      "v_rcp_f32 v81, v81\n"
      "v_mul_f32 v72, v72, v80\n"
      "global_load_dwordx4 v[168:171], v108, s[12:13] offset:2048 sc1\n"
      "s_waitcnt lgkmcnt(5)\n"
      "v_mfma_f32_32x32x16_bf16 v[2:17], v[204:207], v[90:93], v[2:17]\n"
      "ds_read_b64_tr_b16 v[86:87], v100 offset:1024\n"
      "ds_read_b64_tr_b16 v[88:89], v100 offset:1280\n"
      "s_waitcnt lgkmcnt(6)\n"
      "v_max3_u32 v111, v111, v42, v43\n"
      "v_max3_u32 v110, v110, v44, v45\n"
      "s_waitcnt lgkmcnt(5)\n"
      "v_max3_u32 v111, v111, v46, v47\n"
      "v_max3_u32 v110, v110, v48, v49\n"
      "v_max_u32 v111, v111, v110\n"
      "v_cmp_eq_u32 vcc, -1, v111\n"
      "s_nop 0\n"
      "s_cbranch_vccnz L_slow_8040\n"
      "L_ha_8039:\n"
      "v_fma_f32 v81, v81, v104, v106\n"
      "v_mul_f32 v73, v73, v81\n"
      "v_cvt_pk_bf16_f32 v76, v70, v71\n"
      "ds_write_b128 v95, v[30:33] offset:58368\n"
      "s_waitcnt lgkmcnt(3)\n"
      "v_mfma_f32_32x32x16_bf16 v[2:17], v[208:211], v[82:85], v[2:17]\n"
      "v_cvt_pk_bf16_f32 v77, v72, v73\n"
      "s_add_u32 s20, s46, 0\n"
      "s_addc_u32 s21, s47, 0\n"
      "global_store_dwordx4 v108, v[74:77], s[20:21] sc1\n"
      "global_load_dwordx4 v[172:175], v108, s[12:13] offset:3072 sc1\n"
      "s_add_u32 s12, s12, 0x1000\n"
      "s_addc_u32 s13, s13, 0\n"
      "v_mfma_f32_32x32x16_bf16 v[50:65], v[82:85], v[82:85], 0\n"
      "ds_read_b64_tr_b16 v[90:91], v100 offset:2048\n"
      "ds_read_b64_tr_b16 v[92:93], v100 offset:2304\n"
      "s_waitcnt lgkmcnt(2)\n"
      "s_barrier\n"
      "ds_read_b128 v[42:45], v94 offset:1536\n"
      "ds_read_b64 v[46:47], v94 offset:1568\n"
      "ds_read_b128 v[66:69], v96 offset:57344\n"
      "ds_read_b128 v[70:73], v96 offset:58368\n"
      "global_load_dwordx4 v[176:179], v108, s[12:13] offset:0 sc1\n"
      "v_mfma_f32_32x32x16_bf16 v[2:17], v[212:215], v[86:89], v[2:17]\n"
      "s_waitcnt lgkmcnt(3)\n"
      "v_add_f32 v42, v42, v43\n"
      "v_add_f32 v44, v44, v45\n"
      "s_waitcnt lgkmcnt(2)\n"
      "v_add_f32 v46, v46, v47\n"
      "v_add_f32 v42, v42, v44\n"
      "v_mul_f32 v46, s15, v46\n"
      "global_load_dwordx4 v[180:183], v108, s[12:13] offset:1024 sc1\n"
      "v_mfma_f32_32x32x16_bf16 v[50:65], v[86:89], v[86:89], v[50:65]\n"
      "ds_read_b64_tr_b16 v[82:83], v100 offset:3072\n"
      "ds_read_b64_tr_b16 v[84:85], v100 offset:3328\n"
      "v_add_u32 v100, 0xfffe2000, v100\n"
      "v_fma_f32 v42, v42, s15, v98\n"
      "s_waitcnt lgkmcnt(3)\n"
      "v_add_f32 v66, v18, v66\n"
      "v_fma_f32 v42, -v46, v46, v42\n"
      "v_add_f32 v67, v19, v67\n"
      "v_cmp_u_f32 vcc, v42, v42\n"
      "s_nop 0\n"
      "s_cbranch_vccnz L_slow_10042\n"
      "L_nn_10041:\n"
      "v_max_f32 v42, v42, v98\n"
      "v_mfma_f32_32x32x16_bf16 v[2:17], v[216:219], v[90:93], v[2:17]\n"
      "v_add_f32 v68, v20, v68\n"
      "v_sqrt_f32 v42, v42\n"
      "v_add_f32 v69, v21, v69\n"
      "v_cvt_pk_bf16_f32 v38, -v46, -v46\n"
      "s_waitcnt lgkmcnt(2)\n"
      "v_add_f32 v70, v22, v70\n"
      "global_load_dwordx4 v[184:187], v108, s[12:13] offset:2048 sc1\n"
      "v_mfma_f32_32x32x16_bf16 v[50:65], v[90:93], v[90:93], v[50:65]\n"
      "ds_read_b64_tr_b16 v[86:87], v101 offset:0\n"
      "ds_read_b64_tr_b16 v[88:89], v101 offset:256\n"
      "v_cvt_pk_bf16_f32 v39, v42, 0\n"
      "v_add_f32 v71, v23, v71\n"
      "v_lshlrev_b32 v42, 16, v39\n"
      "v_add_f32 v72, v24, v72\n"
      "v_rcp_f32 v106, v42\n"
      "global_load_dwordx4 v[188:191], v108, s[12:13] offset:3072 sc1\n"
      "s_add_u32 s12, s12, 0x1000\n"
      "s_addc_u32 s13, s13, 0\n"
      "s_waitcnt lgkmcnt(2)\n"
      "v_mfma_f32_32x32x16_bf16 v[2:17], v[220:223], v[82:85], v[2:17]\n"
      "v_add_f32 v73, v25, v73\n"
      "v_mul_f32 v105, 0x3fb8aa3b, v106\n"
      "s_nop 0\n"
      "v_mfma_f32_32x32x16_bf16 v[66:81], v[34:37], v[38:41], v[66:81]\n"
      "v_mul_f32 v104, -0.5, v106\n"
      "s_add_u32 m0, s8, 0x16000\n"
      "s_nop 0\n"
      "global_load_lds_dwordx4 v108, s[10:11] offset:0 sc1\n"
      "v_mfma_f32_32x32x16_bf16 v[50:65], v[82:85], v[82:85], v[50:65]\n"
      "ds_read_b64_tr_b16 v[90:91], v101 offset:1024\n"
      "ds_read_b64_tr_b16 v[92:93], v101 offset:1280\n"
      "s_waitcnt lgkmcnt(2)\n"
      "v_mfma_f32_32x32x16_bf16 v[2:17], v[224:227], v[86:89], v[2:17]\n"
      "s_nop 2\n"
      "v_fma_f32 v74, v66, v105, -1.0\n"
      "v_exp_f32 v74, v74\n"
      "v_fma_f32 v75, v67, v105, -1.0\n"
      "v_fmac_f32 v74, v74, v74\n"
      "v_mfma_f32_32x32x16_bf16 v[50:65], v[86:89], v[86:89], v[50:65]\n"
      "ds_read_b64_tr_b16 v[82:83], v101 offset:2048\n"
      "ds_read_b64_tr_b16 v[84:85], v101 offset:2304\n"
      "v_exp_f32 v75, v75\n"
      "v_fma_f32 v76, v68, v105, -1.0\n"
      "v_add_f32 v74, 0.5, v74\n"
      "v_fmac_f32 v75, v75, v75\n"
      "s_add_u32 m0, s8, 0x16000\n"
      "s_nop 0\n"
      "global_load_lds_dwordx4 v108, s[10:11] offset:1024 sc1\n"
      "s_waitcnt lgkmcnt(2)\n"
      "v_mfma_f32_32x32x16_bf16 v[2:17], v[228:231], v[90:93], v[2:17]\n"
      "v_exp_f32 v76, v76\n"
      "v_fma_f32 v77, v69, v105, -1.0\n"
      "v_rcp_f32 v74, v74\n"
      "v_add_f32 v75, 0.5, v75\n"
      "v_mfma_f32_32x32x16_bf16 v[50:65], v[90:93], v[90:93], v[50:65]\n"
      "ds_read_b64_tr_b16 v[86:87], v101 offset:3072\n"
      "ds_read_b64_tr_b16 v[88:89], v101 offset:3328\n"
      "v_add_u32 v101, 0xfffe2000, v101\n"
      "v_fmac_f32 v76, v76, v76\n"
      "v_exp_f32 v77, v77\n"
      "v_fma_f32 v74, v74, v104, v106\n"
      "v_rcp_f32 v75, v75\n"
      "s_waitcnt lgkmcnt(2)\n"
      "v_mfma_f32_32x32x16_bf16 v[2:17], v[232:235], v[82:85], v[2:17]\n"
      "v_add_f32 v76, 0.5, v76\n"
      "v_fmac_f32 v77, v77, v77\n"
      "v_mul_f32 v66, v66, v74\n"
      "v_fma_f32 v75, v75, v104, v106\n"
      "v_mfma_f32_32x32x16_bf16 v[50:65], v[82:85], v[82:85], v[50:65]\n"
      "ds_read_b64_tr_b16 v[90:91], v102 offset:0\n"
      "ds_read_b64_tr_b16 v[92:93], v102 offset:256\n"
      "v_rcp_f32 v76, v76\n"
      "v_add_f32 v77, 0.5, v77\n"
      "v_mul_f32 v67, v67, v75\n"
      "v_fma_f32 v76, v76, v104, v106\n"
      "s_add_u32 m0, s8, 0x16000\n"
      "s_nop 0\n"
      "global_load_lds_dwordx4 v108, s[10:11] offset:2048 sc1\n"
      "s_waitcnt lgkmcnt(2)\n"
      "v_mfma_f32_32x32x16_bf16 v[2:17], v[236:239], v[86:89], v[2:17]\n"
      "v_rcp_f32 v77, v77\n"
      "v_mul_f32 v68, v68, v76\n"
      "v_fma_f32 v77, v77, v104, v106\n"
      "v_mul_f32 v69, v69, v77\n"
      "v_mfma_f32_32x32x16_bf16 v[50:65], v[86:89], v[86:89], v[50:65]\n"
      "ds_read_b64_tr_b16 v[82:83], v102 offset:1024\n"
      "ds_read_b64_tr_b16 v[84:85], v102 offset:1280\n"
      "v_cvt_pk_bf16_f32 v74, v66, v67\n"
      "v_cvt_pk_bf16_f32 v75, v68, v69\n"
      "v_fma_f32 v78, v70, v105, -1.0\n"
      "v_exp_f32 v78, v78\n"
      "s_waitcnt lgkmcnt(2)\n"
      "v_mfma_f32_32x32x16_bf16 v[2:17], v[240:243], v[90:93], v[2:17]\n"
      "ds_read_b64_tr_b16 v[86:87], v102 offset:2048\n"
      "ds_read_b64_tr_b16 v[88:89], v102 offset:2304\n"
      "v_fma_f32 v79, v71, v105, -1.0\n"
      "v_fmac_f32 v78, v78, v78\n"
      "v_exp_f32 v79, v79\n"
      "v_fma_f32 v80, v72, v105, -1.0\n"
      "s_add_u32 m0, s8, 0x16000\n"
      "s_nop 0\n"
      "global_load_lds_dwordx4 v108, s[10:11] offset:3072 sc1\n"
      "s_waitcnt lgkmcnt(2)\n"
      "v_mfma_f32_32x32x16_bf16 v[2:17], v[244:247], v[82:85], v[2:17]\n"
      "ds_read_b64_tr_b16 v[90:91], v102 offset:3072\n"
      "ds_read_b64_tr_b16 v[92:93], v102 offset:3328\n"
      "v_add_u32 v102, 0xfffe2000, v102\n"
      "v_add_f32 v78, 0.5, v78\n"
      "v_fmac_f32 v79, v79, v79\n"
      "v_exp_f32 v80, v80\n"
      "v_fma_f32 v81, v73, v105, -1.0\n"
      "v_cndmask_b32 v50, v50, v51, s[50:51]\n"
      "v_cndmask_b32 v52, v52, v53, s[50:51]\n"
      "v_cndmask_b32 v54, v54, v55, s[50:51]\n"
      "v_cndmask_b32 v56, v56, v57, s[50:51]\n"
      "v_cndmask_b32 v58, v58, v59, s[50:51]\n"
      "v_cndmask_b32 v60, v60, v61, s[50:51]\n"
      "s_waitcnt lgkmcnt(2)\n"
      "v_mfma_f32_32x32x16_bf16 v[2:17], v[248:251], v[86:89], v[2:17]\n"
      "ds_read_b64_tr_b16 v[82:83], v99 offset:0\n"
      "ds_read_b64_tr_b16 v[84:85], v99 offset:256\n"
      "v_rcp_f32 v78, v78\n"
      "v_add_f32 v79, 0.5, v79\n"
      "v_fmac_f32 v80, v80, v80\n"
      "v_exp_f32 v81, v81\n"
      "v_cndmask_b32 v62, v62, v63, s[50:51]\n"
      "v_cndmask_b32 v64, v64, v65, s[50:51]\n"
      "v_cndmask_b32 v50, v50, v52, s[52:53]\n"
      "v_cndmask_b32 v54, v54, v56, s[52:53]\n"
      "v_cndmask_b32 v58, v58, v60, s[52:53]\n"
      "s_waitcnt lgkmcnt(2)\n"
      "v_mfma_f32_32x32x16_bf16 v[2:17], v[252:255], v[90:93], v[2:17]\n"
      "ds_read_b64_tr_b16 v[86:87], v99 offset:1024\n"
      "ds_read_b64_tr_b16 v[88:89], v99 offset:1280\n"
      "v_fma_f32 v78, v78, v104, v106\n"
      "v_rcp_f32 v79, v79\n"
      "v_add_f32 v80, 0.5, v80\n"
      "v_fmac_f32 v81, v81, v81\n"
      "v_cndmask_b32 v62, v62, v64, s[52:53]\n"
      "v_cndmask_b32 v50, v50, v54, s[58:59]\n"
      "v_cndmask_b32 v58, v58, v62, s[58:59]\n"
      "v_cndmask_b32 v50, v50, v58, s[60:61]\n"
      "s_mov_b64 exec, s[62:63]\n"
      "ds_write_b32 v97, v50 offset:0\n"
      "s_mov_b64 exec, -1\n"
      "s_waitcnt lgkmcnt(3)\n"
      "v_mfma_f32_32x32x16_bf16 v[18:33], v[192:195], v[82:85], 0\n"
      "ds_read_b64_tr_b16 v[90:91], v99 offset:2048\n"
      "ds_read_b64_tr_b16 v[92:93], v99 offset:2304\n"
      "v_mul_f32 v70, v70, v78\n"
      "v_fma_f32 v79, v79, v104, v106\n"
      "v_rcp_f32 v80, v80\n"
      "v_add_f32 v81, 0.5, v81\n"
      "v_mul_f32 v71, v71, v79\n"
      "s_waitcnt lgkmcnt(3)\n"
      "v_mfma_f32_32x32x16_bf16 v[18:33], v[196:199], v[86:89], v[18:33]\n"
      "ds_read_b64_tr_b16 v[82:83], v99 offset:3072\n"
      "ds_read_b64_tr_b16 v[84:85], v99 offset:3328\n"
      "v_fma_f32 v80, v80, v104, v106\n"
      "v_rcp_f32 v81, v81\n"
      "v_mul_f32 v72, v72, v80\n"
      "v_fma_f32 v81, v81, v104, v106\n"
      "ds_write_b128 v95, v[10:13] offset:32768\n"
      "s_waitcnt lgkmcnt(3)\n"
      "v_mfma_f32_32x32x16_bf16 v[18:33], v[200:203], v[90:93], v[18:33]\n"
      "ds_read_b64_tr_b16 v[86:87], v100 offset:0\n"
      "ds_read_b64_tr_b16 v[88:89], v100 offset:256\n"
      "v_mul_f32 v73, v73, v81\n"
      "v_cvt_pk_bf16_f32 v76, v70, v71\n"
      "v_cvt_pk_bf16_f32 v77, v72, v73\n"
      "s_add_u32 s20, s46, 0x1c000\n"
      "s_addc_u32 s21, s47, 0\n"
      "global_store_dwordx4 v108, v[74:77], s[20:21] sc1\n"
      "ds_write_b128 v95, v[14:17] offset:33792\n"
      "s_waitcnt lgkmcnt(4)\n"
      "v_mfma_f32_32x32x16_bf16 v[18:33], v[204:207], v[82:85], v[18:33]\n"
      "ds_read_b64_tr_b16 v[90:91], v100 offset:1024\n"
      "ds_read_b64_tr_b16 v[92:93], v100 offset:1280\n"
      "s_waitcnt lgkmcnt(2)\n"
      "s_barrier\n"
      "ds_read_b128 v[42:45], v94 offset:0\n"
      "ds_read_b64 v[46:47], v94 offset:32\n"
      "ds_read_b128 v[66:69], v96 offset:32768\n"
      "ds_read_b128 v[70:73], v96 offset:33792\n"
      "v_mfma_f32_32x32x16_bf16 v[18:33], v[208:211], v[86:89], v[18:33]\n"
      "ds_read_b64_tr_b16 v[82:83], v100 offset:2048\n"
      "ds_read_b64_tr_b16 v[84:85], v100 offset:2304\n"
      "s_waitcnt lgkmcnt(5)\n"
      "v_add_f32 v42, v42, v43\n"
      "v_add_f32 v44, v44, v45\n"
      "s_waitcnt lgkmcnt(4)\n"
      "v_add_f32 v46, v46, v47\n"
      "v_add_f32 v42, v42, v44\n"
      "v_mul_f32 v46, s15, v46\n"
      "v_fma_f32 v42, v42, s15, v98\n"
      "s_waitcnt lgkmcnt(3)\n"
      "v_add_f32 v66, v2, v66\n"
      "v_fma_f32 v42, -v46, v46, v42\n"
      "v_mfma_f32_32x32x16_bf16 v[18:33], v[212:215], v[90:93], v[18:33]\n"
      "ds_read_b64_tr_b16 v[86:87], v100 offset:3072\n"
      "ds_read_b64_tr_b16 v[88:89], v100 offset:3328\n"
      "v_add_f32 v67, v3, v67\n"
      "v_cmp_u_f32 vcc, v42, v42\n"
      "s_nop 0\n"
      "s_cbranch_vccnz L_slow_12044\n"
      "L_nn_12043:\n"
      "v_max_f32 v42, v42, v98\n"
      "v_add_f32 v68, v4, v68\n"
      "v_sqrt_f32 v42, v42\n"
      "v_add_f32 v69, v5, v69\n"
      "v_cvt_pk_bf16_f32 v38, -v46, -v46\n"
      "s_waitcnt lgkmcnt(4)\n"
      "v_add_f32 v70, v6, v70\n"
      "v_cvt_pk_bf16_f32 v39, v42, 0\n"
      "s_waitcnt lgkmcnt(2)\n"
      "v_mfma_f32_32x32x16_bf16 v[18:33], v[216:219], v[82:85], v[18:33]\n"
      "ds_read_b64_tr_b16 v[90:91], v101 offset:0\n"
      "ds_read_b64_tr_b16 v[92:93], v101 offset:256\n"
      "v_add_f32 v71, v7, v71\n"
      "v_lshlrev_b32 v42, 16, v39\n"
      "v_add_f32 v72, v8, v72\n"
      "v_rcp_f32 v106, v42\n"
      "v_add_f32 v73, v9, v73\n"
      "v_mul_f32 v105, 0x3fb8aa3b, v106\n"
      "s_nop 0\n"
      "v_mfma_f32_32x32x16_bf16 v[66:81], v[34:37], v[38:41], v[66:81]\n"
      "v_mul_f32 v104, -0.5, v106\n"
      "global_load_dwordx4 v[208:211], v108, s[28:29] offset:0 sc1\n"
      "s_waitcnt lgkmcnt(2)\n"
      "v_mfma_f32_32x32x16_bf16 v[18:33], v[220:223], v[86:89], v[18:33]\n"
      "ds_read_b64_tr_b16 v[82:83], v101 offset:1024\n"
      "ds_read_b64_tr_b16 v[84:85], v101 offset:1280\n"
      "global_load_dwordx4 v[212:215], v108, s[28:29] offset:1024 sc1\n"
      "s_waitcnt lgkmcnt(2)\n"
      "v_mfma_f32_32x32x16_bf16 v[18:33], v[224:227], v[90:93], v[18:33]\n"
      "ds_read_b64_tr_b16 v[86:87], v101 offset:2048\n"
      "ds_read_b64_tr_b16 v[88:89], v101 offset:2304\n"
      "s_nop 0\n"
      "v_fma_f32 v74, v66, v105, -1.0\n"
      "v_exp_f32 v74, v74\n"
      "v_fma_f32 v75, v67, v105, -1.0\n"
      "v_fmac_f32 v74, v74, v74\n"
      "v_exp_f32 v75, v75\n"
      "v_fma_f32 v76, v68, v105, -1.0\n"
      "v_add_f32 v74, 0.5, v74\n"
      "v_fmac_f32 v75, v75, v75\n"
      "global_load_dwordx4 v[224:227], v1, s[70:71] offset:-4096 sc1\n"
      "s_waitcnt lgkmcnt(2)\n"
      "v_mfma_f32_32x32x16_bf16 v[18:33], v[228:231], v[82:85], v[18:33]\n"
      "ds_read_b64_tr_b16 v[90:91], v101 offset:3072\n"
      "ds_read_b64_tr_b16 v[92:93], v101 offset:3328\n"
      "v_exp_f32 v76, v76\n"
      "v_fma_f32 v77, v69, v105, -1.0\n"
      "v_rcp_f32 v74, v74\n"
      "v_add_f32 v75, 0.5, v75\n"
      "v_fmac_f32 v76, v76, v76\n"
      "v_exp_f32 v77, v77\n"
      "v_fma_f32 v74, v74, v104, v106\n"
      "v_rcp_f32 v75, v75\n"
      "global_load_dwordx4 v[228:231], v1, s[70:71] offset:-2048 sc1\n"
      "s_waitcnt lgkmcnt(2)\n"
      "v_mfma_f32_32x32x16_bf16 v[18:33], v[232:235], v[86:89], v[18:33]\n"
      "ds_read_b64_tr_b16 v[82:83], v102 offset:0\n"
      "ds_read_b64_tr_b16 v[84:85], v102 offset:256\n"
      "v_add_f32 v76, 0.5, v76\n"
      "v_fmac_f32 v77, v77, v77\n"
      "v_mul_f32 v66, v66, v74\n"
      "v_fma_f32 v75, v75, v104, v106\n"
      "v_rcp_f32 v76, v76\n"
      "v_add_f32 v77, 0.5, v77\n"
      "v_mul_f32 v67, v67, v75\n"
      "global_load_dwordx4 v[232:235], v1, s[70:71] offset:0 sc1\n"
      "s_waitcnt lgkmcnt(2)\n"
      "v_mfma_f32_32x32x16_bf16 v[18:33], v[236:239], v[90:93], v[18:33]\n"
      "ds_read_b64_tr_b16 v[86:87], v102 offset:1024\n"
      "ds_read_b64_tr_b16 v[88:89], v102 offset:1280\n"
      "v_fma_f32 v76, v76, v104, v106\n"
      "v_rcp_f32 v77, v77\n"
      "v_mul_f32 v68, v68, v76\n"
      "v_fma_f32 v77, v77, v104, v106\n"
      "v_mul_f32 v69, v69, v77\n"
      "v_cvt_pk_bf16_f32 v74, v66, v67\n"
      "v_cvt_pk_bf16_f32 v75, v68, v69\n"
      "v_fma_f32 v78, v70, v105, -1.0\n"
      "global_load_dwordx4 v[236:239], v1, s[70:71] offset:2048 sc1\n"
      "s_waitcnt lgkmcnt(2)\n"
      "v_mfma_f32_32x32x16_bf16 v[18:33], v[240:243], v[82:85], v[18:33]\n"
      "ds_read_b64_tr_b16 v[90:91], v102 offset:2048\n"
      "ds_read_b64_tr_b16 v[92:93], v102 offset:2304\n"
      "v_exp_f32 v78, v78\n"
      "v_fma_f32 v79, v71, v105, -1.0\n"
      "v_fmac_f32 v78, v78, v78\n"
      "v_exp_f32 v79, v79\n"
      "v_fma_f32 v80, v72, v105, -1.0\n"
      "v_add_f32 v78, 0.5, v78\n"
      "v_fmac_f32 v79, v79, v79\n"
      "v_exp_f32 v80, v80\n"
      "s_waitcnt vmcnt(7)\n"
      "s_add_u32 s43, s8, 0x16000\n"
      "v_add_u32 v109, s43, v108\n"
      "ds_read_b128 v[192:195], v109 offset:0\n"
      "ds_read_b128 v[196:199], v109 offset:1024\n"
      "ds_read_b128 v[200:203], v109 offset:2048\n"
      "ds_read_b128 v[204:207], v109 offset:3072\n"
      "s_lshr_b32 s43, s8, 1\n"
      "s_add_u32 s43, s43, 0x12000\n"
      "v_add_u32 v109, s43, v108\n"
      "ds_read_b128 v[216:219], v109 offset:0\n"
      "ds_read_b128 v[220:223], v109 offset:1024\n"
      "global_load_dwordx4 v[240:243], v103, s[70:71] offset:-4096 sc1\n"
      "s_waitcnt lgkmcnt(8)\n"
      "v_mfma_f32_32x32x16_bf16 v[18:33], v[244:247], v[86:89], v[18:33]\n"
      "ds_read_b64_tr_b16 v[82:83], v102 offset:3072\n"
      "ds_read_b64_tr_b16 v[84:85], v102 offset:3328\n"
      "v_fma_f32 v81, v73, v105, -1.0\n"
      "v_rcp_f32 v78, v78\n"
      "v_add_f32 v79, 0.5, v79\n"
      "v_fmac_f32 v80, v80, v80\n"
      "v_exp_f32 v81, v81\n"
      "v_fma_f32 v78, v78, v104, v106\n"
      "v_rcp_f32 v79, v79\n"
      "global_load_dwordx4 v[244:247], v103, s[70:71] offset:-2048 sc1\n"
      "s_waitcnt lgkmcnt(8)\n"
      "v_mfma_f32_32x32x16_bf16 v[18:33], v[248:251], v[90:93], v[18:33]\n"
      "v_add_f32 v80, 0.5, v80\n"
      "v_fmac_f32 v81, v81, v81\n"
      "v_mul_f32 v70, v70, v78\n"
      "v_fma_f32 v79, v79, v104, v106\n"
      "v_rcp_f32 v80, v80\n"
      "v_add_f32 v81, 0.5, v81\n"
      "v_mul_f32 v71, v71, v79\n"
      "v_fma_f32 v80, v80, v104, v106\n"
      "global_load_dwordx4 v[248:251], v103, s[70:71] offset:0 sc1\n"
      "s_waitcnt lgkmcnt(0)\n"
      "v_mfma_f32_32x32x16_bf16 v[18:33], v[252:255], v[82:85], v[18:33]\n"
      "v_rcp_f32 v81, v81\n"
      "v_mul_f32 v72, v72, v80\n"
      "v_fma_f32 v81, v81, v104, v106\n"
      "v_mul_f32 v73, v73, v81\n"
      "v_cvt_pk_bf16_f32 v76, v70, v71\n"
      "v_cvt_pk_bf16_f32 v77, v72, v73\n"
      "s_add_u32 s20, s46, 0x38000\n"
      "s_addc_u32 s21, s47, 0\n"
      "global_store_dwordx4 v108, v[74:77], s[20:21] sc1\n"
      "s_branch L_end_14045\n"
      "L_skip_8035:\n"
      "ds_read_b64_tr_b16 v[82:83], v99 offset:0\n"
      "ds_read_b64_tr_b16 v[84:85], v99 offset:256\n"
      "ds_read_b64_tr_b16 v[86:87], v99 offset:1024\n"
      "ds_read_b64_tr_b16 v[88:89], v99 offset:1280\n"
      "s_waitcnt lgkmcnt(2)\n"
      "v_mfma_f32_32x32x16_bf16 v[2:17], v[192:195], v[82:85], 0\n"
      "ds_read_b64_tr_b16 v[90:91], v99 offset:2048\n"
      "ds_read_b64_tr_b16 v[92:93], v99 offset:2304\n"
      "s_add_u32 m0, s8, 0x1e000\n"
      "s_nop 0\n"
      "global_load_lds_dwordx4 v108, s[44:45] offset:0 sc1\n"
      "s_waitcnt lgkmcnt(2)\n"
      "v_mfma_f32_32x32x16_bf16 v[2:17], v[196:199], v[86:89], v[2:17]\n"
      "ds_read_b64_tr_b16 v[82:83], v99 offset:3072\n"
      "ds_read_b64_tr_b16 v[84:85], v99 offset:3328\n"
      "v_add_u32 v99, 0x8000, v99\n"
      "global_load_lds_dwordx4 v108, s[44:45] offset:1024 sc1\n"
      "s_waitcnt lgkmcnt(2)\n"
      "v_mfma_f32_32x32x16_bf16 v[2:17], v[200:203], v[90:93], v[2:17]\n"
      "ds_read_b64_tr_b16 v[86:87], v100 offset:0\n"
      "ds_read_b64_tr_b16 v[88:89], v100 offset:256\n"
      "global_load_lds_dwordx4 v108, s[44:45] offset:2048 sc1\n"
      "s_waitcnt lgkmcnt(2)\n"
      "v_mfma_f32_32x32x16_bf16 v[2:17], v[204:207], v[82:85], v[2:17]\n"
      "ds_read_b64_tr_b16 v[90:91], v100 offset:1024\n"
      "ds_read_b64_tr_b16 v[92:93], v100 offset:1280\n"
      "global_load_lds_dwordx4 v108, s[44:45] offset:3072 sc1\n"
      "s_waitcnt lgkmcnt(2)\n"
      "v_mfma_f32_32x32x16_bf16 v[2:17], v[208:211], v[86:89], v[2:17]\n"
      "ds_read_b64_tr_b16 v[82:83], v100 offset:2048\n"
      "ds_read_b64_tr_b16 v[84:85], v100 offset:2304\n"
      "s_add_u32 m0, s8, 0\n"
      "s_nop 0\n"
      "global_load_lds_dwordx4 v108, s[48:49] offset:0 sc1\n"
      "s_waitcnt lgkmcnt(2)\n"
      "v_mfma_f32_32x32x16_bf16 v[2:17], v[212:215], v[90:93], v[2:17]\n"
      "ds_read_b64_tr_b16 v[86:87], v100 offset:3072\n"
      "ds_read_b64_tr_b16 v[88:89], v100 offset:3328\n"
      "v_add_u32 v100, 0x8000, v100\n"
      "global_load_lds_dwordx4 v108, s[48:49] offset:1024 sc1\n"
      "s_waitcnt lgkmcnt(2)\n"
      "v_mfma_f32_32x32x16_bf16 v[2:17], v[216:219], v[82:85], v[2:17]\n"
      "ds_read_b64_tr_b16 v[90:91], v101 offset:0\n"
      "ds_read_b64_tr_b16 v[92:93], v101 offset:256\n"
      "global_load_lds_dwordx4 v108, s[48:49] offset:2048 sc1\n"
      "s_waitcnt lgkmcnt(2)\n"
      "v_mfma_f32_32x32x16_bf16 v[2:17], v[220:223], v[86:89], v[2:17]\n"
      "ds_read_b64_tr_b16 v[82:83], v101 offset:1024\n"
      "ds_read_b64_tr_b16 v[84:85], v101 offset:1280\n"
      "global_load_lds_dwordx4 v108, s[48:49] offset:3072 sc1\n"
      "s_waitcnt lgkmcnt(2)\n"
      "v_mfma_f32_32x32x16_bf16 v[2:17], v[224:227], v[90:93], v[2:17]\n"
      "ds_read_b64_tr_b16 v[86:87], v101 offset:2048\n"
      "ds_read_b64_tr_b16 v[88:89], v101 offset:2304\n"
      "s_waitcnt lgkmcnt(2)\n"
      "v_mfma_f32_32x32x16_bf16 v[2:17], v[228:231], v[82:85], v[2:17]\n"
      "ds_read_b64_tr_b16 v[90:91], v101 offset:3072\n"
      "ds_read_b64_tr_b16 v[92:93], v101 offset:3328\n"
      "v_add_u32 v101, 0x8000, v101\n"
      "s_waitcnt vmcnt(8)\n"
      "s_barrier\n"
      "s_waitcnt lgkmcnt(2)\n"
      "v_mfma_f32_32x32x16_bf16 v[2:17], v[232:235], v[86:89], v[2:17]\n"
      "ds_read_b64_tr_b16 v[82:83], v102 offset:0\n"
      "ds_read_b64_tr_b16 v[84:85], v102 offset:256\n"
      "s_waitcnt lgkmcnt(2)\n"
      "v_mfma_f32_32x32x16_bf16 v[2:17], v[236:239], v[90:93], v[2:17]\n"
      "ds_read_b64_tr_b16 v[86:87], v102 offset:1024\n"
      "ds_read_b64_tr_b16 v[88:89], v102 offset:1280\n"
      "global_load_dwordx4 v[112:115], v108, s[12:13] offset:0 sc1\n"
      "s_waitcnt lgkmcnt(2)\n"
      "v_mfma_f32_32x32x16_bf16 v[2:17], v[240:243], v[82:85], v[2:17]\n"
      "ds_read_b64_tr_b16 v[90:91], v102 offset:2048\n"
      "ds_read_b64_tr_b16 v[92:93], v102 offset:2304\n"
      "global_load_dwordx4 v[116:119], v108, s[12:13] offset:1024 sc1\n"
      "s_waitcnt lgkmcnt(2)\n"
      "v_mfma_f32_32x32x16_bf16 v[2:17], v[244:247], v[86:89], v[2:17]\n"
      "ds_read_b64_tr_b16 v[82:83], v102 offset:3072\n"
      "ds_read_b64_tr_b16 v[84:85], v102 offset:3328\n"
      "v_add_u32 v102, 0x8000, v102\n"
      "global_load_dwordx4 v[120:123], v108, s[12:13] offset:2048 sc1\n"
      "s_waitcnt lgkmcnt(2)\n"
      "v_mfma_f32_32x32x16_bf16 v[2:17], v[248:251], v[90:93], v[2:17]\n"
      "ds_read_b64_tr_b16 v[86:87], v99 offset:0\n"
      "ds_read_b64_tr_b16 v[88:89], v99 offset:256\n"
      "global_load_dwordx4 v[124:127], v108, s[12:13] offset:3072 sc1\n"
      "s_add_u32 s12, s12, 0x1000\n"
      "s_addc_u32 s13, s13, 0\n"
      "s_waitcnt lgkmcnt(2)\n"
      "v_mfma_f32_32x32x16_bf16 v[2:17], v[252:255], v[82:85], v[2:17]\n"
      "ds_read_b64_tr_b16 v[90:91], v99 offset:1024\n"
      "ds_read_b64_tr_b16 v[92:93], v99 offset:1280\n"
      "s_waitcnt lgkmcnt(2)\n"
      "v_mfma_f32_32x32x16_bf16 v[18:33], v[192:195], v[86:89], 0\n"
      "ds_read_b64_tr_b16 v[82:83], v99 offset:2048\n"
      "ds_read_b64_tr_b16 v[84:85], v99 offset:2304\n"
      "global_load_dwordx4 v[128:131], v108, s[12:13] offset:0 sc1\n"
      "s_waitcnt lgkmcnt(2)\n"
      "v_mfma_f32_32x32x16_bf16 v[18:33], v[196:199], v[90:93], v[18:33]\n"
      "ds_read_b64_tr_b16 v[86:87], v99 offset:3072\n"
      "ds_read_b64_tr_b16 v[88:89], v99 offset:3328\n"
      "v_add_u32 v99, 0x8000, v99\n"
      "ds_write_b128 v95, v[10:13] offset:32768\n"
      "s_waitcnt lgkmcnt(3)\n"
      "v_mfma_f32_32x32x16_bf16 v[18:33], v[200:203], v[82:85], v[18:33]\n"
      "ds_read_b64_tr_b16 v[90:91], v100 offset:0\n"
      "ds_read_b64_tr_b16 v[92:93], v100 offset:256\n"
      "ds_write_b128 v95, v[14:17] offset:33792\n"
      "global_load_dwordx4 v[132:135], v108, s[12:13] offset:1024 sc1\n"
      "s_waitcnt lgkmcnt(4)\n"
      "v_mfma_f32_32x32x16_bf16 v[18:33], v[204:207], v[86:89], v[18:33]\n"
      "ds_read_b64_tr_b16 v[82:83], v100 offset:1024\n"
      "ds_read_b64_tr_b16 v[84:85], v100 offset:1280\n"
      "s_waitcnt lgkmcnt(3)\n"
      "v_mfma_f32_32x32x16_bf16 v[18:33], v[208:211], v[90:93], v[18:33]\n"
      "s_cmp_eq_u32 s33, 6\n"
      "s_cbranch_scc0 L_skip_14046\n"
      "s_mov_b32 exec_hi, 0\n"
      "ds_write_b32 v94, v10 offset:32\n"
      "s_mov_b64 exec, -1\n"
      "L_skip_14046:\n"
      "s_cmp_eq_u32 s33, 7\n"
      "s_cbranch_scc0 L_skip_14047\n"
      "s_mov_b32 exec_hi, 0\n"
      "ds_write_b32 v94, v2 offset:36\n"
      "s_mov_b64 exec, -1\n"
      "L_skip_14047:\n"
      "v_mfma_f32_32x32x16_bf16 v[50:65], v[90:93], v[90:93], 0\n"
      "ds_read_b64_tr_b16 v[86:87], v100 offset:2048\n"
      "ds_read_b64_tr_b16 v[88:89], v100 offset:2304\n"
      "global_load_dwordx4 v[136:139], v108, s[12:13] offset:2048 sc1\n"
      "s_waitcnt lgkmcnt(2)\n"
      "v_mfma_f32_32x32x16_bf16 v[18:33], v[212:215], v[82:85], v[18:33]\n"
      "s_waitcnt vmcnt(11)\n"
      "s_barrier\n"
      "ds_read_b128 v[42:45], v94 offset:0\n"
      "ds_read_b64 v[46:47], v94 offset:32\n"
      "ds_read_b128 v[66:69], v96 offset:32768\n"
      "ds_read_b128 v[70:73], v96 offset:33792\n"
      "v_mfma_f32_32x32x16_bf16 v[50:65], v[82:85], v[82:85], v[50:65]\n"
      "ds_read_b64_tr_b16 v[90:91], v100 offset:3072\n"
      "ds_read_b64_tr_b16 v[92:93], v100 offset:3328\n"
      "v_add_u32 v100, 0x8000, v100\n"
      "s_waitcnt lgkmcnt(5)\n"
      "v_add_f32 v42, v42, v43\n"
      "v_add_f32 v44, v44, v45\n"
      "s_waitcnt lgkmcnt(4)\n"
      "v_add_f32 v46, v46, v47\n"
      "v_add_f32 v42, v42, v44\n"
      "v_mul_f32 v46, s15, v46\n"
      "s_lshr_b32 s43, s8, 1\n"
      "s_add_u32 m0, s43, 0x11800\n"
      "s_nop 0\n"
      "global_load_lds_dwordx4 v108, s[28:29] offset:2048 sc1\n"
      "v_mfma_f32_32x32x16_bf16 v[18:33], v[216:219], v[86:89], v[18:33]\n"
      "v_fma_f32 v42, v42, s15, v98\n"
      "s_waitcnt lgkmcnt(3)\n"
      "v_add_f32 v66, v2, v66\n"
      "v_fma_f32 v42, -v46, v46, v42\n"
      "v_add_f32 v67, v3, v67\n"
      "v_max_f32 v42, v42, v98\n"
      "global_load_dwordx4 v[140:143], v108, s[12:13] offset:3072 sc1\n"
      "s_add_u32 s12, s12, 0x1000\n"
      "s_addc_u32 s13, s13, 0\n"
      "v_mfma_f32_32x32x16_bf16 v[50:65], v[86:89], v[86:89], v[50:65]\n"
      "ds_read_b64_tr_b16 v[82:83], v101 offset:0\n"
      "ds_read_b64_tr_b16 v[84:85], v101 offset:256\n"
      "v_add_f32 v68, v4, v68\n"
      "v_sqrt_f32 v42, v42\n"
      "v_add_f32 v69, v5, v69\n"
      "v_cvt_pk_bf16_f32 v38, -v46, -v46\n"
      "s_waitcnt lgkmcnt(4)\n"
      "v_add_f32 v70, v6, v70\n"
      "s_lshr_b32 s43, s8, 1\n"
      "s_add_u32 m0, s43, 0x11800\n"
      "s_nop 0\n"
      "global_load_lds_dwordx4 v108, s[28:29] offset:3072 sc1\n"
      "s_waitcnt lgkmcnt(2)\n"
      "v_mfma_f32_32x32x16_bf16 v[18:33], v[220:223], v[90:93], v[18:33]\n"
      "v_cvt_pk_bf16_f32 v39, v42, 0\n"
      "v_add_f32 v71, v7, v71\n"
      "v_lshlrev_b32 v42, 16, v39\n"
      "v_add_f32 v72, v8, v72\n"
      "v_rcp_f32 v106, v42\n"
      "v_mfma_f32_32x32x16_bf16 v[50:65], v[90:93], v[90:93], v[50:65]\n"
      "ds_read_b64_tr_b16 v[86:87], v101 offset:1024\n"
      "ds_read_b64_tr_b16 v[88:89], v101 offset:1280\n"
      "v_add_f32 v73, v9, v73\n"
      "v_mul_f32 v105, 0x3fb8aa3b, v106\n"
      "s_nop 0\n"
      "v_mfma_f32_32x32x16_bf16 v[66:81], v[34:37], v[38:41], v[66:81]\n"
      "v_mul_f32 v104, -0.5, v106\n"
      "global_load_dwordx4 v[144:147], v108, s[12:13] offset:0 sc1\n"
      "s_waitcnt lgkmcnt(2)\n"
      "v_mfma_f32_32x32x16_bf16 v[18:33], v[224:227], v[82:85], v[18:33]\n"
      "v_mfma_f32_32x32x16_bf16 v[50:65], v[82:85], v[82:85], v[50:65]\n"
      "ds_read_b64_tr_b16 v[90:91], v101 offset:2048\n"
      "ds_read_b64_tr_b16 v[92:93], v101 offset:2304\n"
      "s_nop 4\n"
      "v_fma_f32 v74, v66, v105, -1.0\n"
      "v_exp_f32 v74, v74\n"
      "v_fma_f32 v75, v67, v105, -1.0\n"
      "v_fmac_f32 v74, v74, v74\n"
      "v_exp_f32 v75, v75\n"
      "s_waitcnt lgkmcnt(2)\n"
      "v_mfma_f32_32x32x16_bf16 v[18:33], v[228:231], v[86:89], v[18:33]\n"
      "v_fma_f32 v76, v68, v105, -1.0\n"
      "v_add_f32 v74, 0.5, v74\n"
      "v_fmac_f32 v75, v75, v75\n"
      "v_exp_f32 v76, v76\n"
      "global_load_dwordx4 v[148:151], v108, s[12:13] offset:1024 sc1\n"
      "v_mfma_f32_32x32x16_bf16 v[50:65], v[86:89], v[86:89], v[50:65]\n"
      "ds_read_b64_tr_b16 v[82:83], v101 offset:3072\n"
      "ds_read_b64_tr_b16 v[84:85], v101 offset:3328\n"
      "v_add_u32 v101, 0x8000, v101\n"
      "v_fma_f32 v77, v69, v105, -1.0\n"
      "v_rcp_f32 v74, v74\n"
      "v_add_f32 v75, 0.5, v75\n"
      "v_fmac_f32 v76, v76, v76\n"
      "v_exp_f32 v77, v77\n"
      "s_waitcnt lgkmcnt(2)\n"
      "v_mfma_f32_32x32x16_bf16 v[18:33], v[232:235], v[90:93], v[18:33]\n"
      "v_fma_f32 v74, v74, v104, v106\n"
      "v_rcp_f32 v75, v75\n"
      "v_add_f32 v76, 0.5, v76\n"
      "v_fmac_f32 v77, v77, v77\n"
      "v_mfma_f32_32x32x16_bf16 v[50:65], v[90:93], v[90:93], v[50:65]\n"
      "ds_read_b64_tr_b16 v[86:87], v102 offset:0\n"
      "ds_read_b64_tr_b16 v[88:89], v102 offset:256\n"
      "v_mul_f32 v66, v66, v74\n"
      "v_fma_f32 v75, v75, v104, v106\n"
      "v_rcp_f32 v76, v76\n"
      "v_add_f32 v77, 0.5, v77\n"
      "global_load_dwordx4 v[152:155], v108, s[12:13] offset:2048 sc1\n"
      "s_waitcnt lgkmcnt(2)\n"
      "v_mfma_f32_32x32x16_bf16 v[18:33], v[236:239], v[82:85], v[18:33]\n"
      "v_mul_f32 v67, v67, v75\n"
      "v_fma_f32 v76, v76, v104, v106\n"
      "v_rcp_f32 v77, v77\n"
      "v_mul_f32 v68, v68, v76\n"
      "v_fma_f32 v77, v77, v104, v106\n"
      "v_mfma_f32_32x32x16_bf16 v[50:65], v[82:85], v[82:85], v[50:65]\n"
      "ds_read_b64_tr_b16 v[90:91], v102 offset:1024\n"
      "ds_read_b64_tr_b16 v[92:93], v102 offset:1280\n"
      "v_mul_f32 v69, v69, v77\n"
      "v_cvt_pk_bf16_f32 v74, v66, v67\n"
      "v_cvt_pk_bf16_f32 v75, v68, v69\n"
      "v_fma_f32 v78, v70, v105, -1.0\n"
      "s_waitcnt lgkmcnt(2)\n"
      "v_mfma_f32_32x32x16_bf16 v[18:33], v[240:243], v[86:89], v[18:33]\n"
      "ds_read_b64_tr_b16 v[82:83], v102 offset:2048\n"
      "ds_read_b64_tr_b16 v[84:85], v102 offset:2304\n"
      "v_exp_f32 v78, v78\n"
      "v_fma_f32 v79, v71, v105, -1.0\n"
      "v_fmac_f32 v78, v78, v78\n"
      "v_exp_f32 v79, v79\n"
      "global_load_dwordx4 v[156:159], v108, s[12:13] offset:3072 sc1\n"
      "s_add_u32 s12, s12, 0x1000\n"
      "s_addc_u32 s13, s13, 0\n"
      "s_waitcnt lgkmcnt(2)\n"
      "v_mfma_f32_32x32x16_bf16 v[18:33], v[244:247], v[90:93], v[18:33]\n"
      "ds_read_b64_tr_b16 v[86:87], v102 offset:3072\n"
      "ds_read_b64_tr_b16 v[88:89], v102 offset:3328\n"
      "v_add_u32 v102, 0x8000, v102\n"
      "v_fma_f32 v80, v72, v105, -1.0\n"
      "v_add_f32 v78, 0.5, v78\n"
      "v_fmac_f32 v79, v79, v79\n"
      "v_exp_f32 v80, v80\n"
      "v_fma_f32 v81, v73, v105, -1.0\n"
      "v_cndmask_b32 v50, v50, v51, s[50:51]\n"
      "v_cndmask_b32 v52, v52, v53, s[50:51]\n"
      "v_cndmask_b32 v54, v54, v55, s[50:51]\n"
      "v_cndmask_b32 v56, v56, v57, s[50:51]\n"
      "v_cndmask_b32 v58, v58, v59, s[50:51]\n"
      "v_cndmask_b32 v60, v60, v61, s[50:51]\n"
      "s_waitcnt lgkmcnt(2)\n"
      "v_mfma_f32_32x32x16_bf16 v[18:33], v[248:251], v[82:85], v[18:33]\n"
      "ds_read_b64_tr_b16 v[90:91], v99 offset:0\n"
      "ds_read_b64_tr_b16 v[92:93], v99 offset:256\n"
      "v_rcp_f32 v78, v78\n"
      "v_add_f32 v79, 0.5, v79\n"
      "v_fmac_f32 v80, v80, v80\n"
      "v_exp_f32 v81, v81\n"
      "v_cndmask_b32 v62, v62, v63, s[50:51]\n"
      "v_cndmask_b32 v64, v64, v65, s[50:51]\n"
      "v_cndmask_b32 v50, v50, v52, s[52:53]\n"
      "v_cndmask_b32 v54, v54, v56, s[52:53]\n"
      "v_cndmask_b32 v58, v58, v60, s[52:53]\n"
      "s_waitcnt lgkmcnt(2)\n"
      "v_mfma_f32_32x32x16_bf16 v[18:33], v[252:255], v[86:89], v[18:33]\n"
      "ds_read_b64_tr_b16 v[82:83], v99 offset:1024\n"
      "ds_read_b64_tr_b16 v[84:85], v99 offset:1280\n"
      "v_fma_f32 v78, v78, v104, v106\n"
      "v_rcp_f32 v79, v79\n"
      "v_add_f32 v80, 0.5, v80\n"
      "v_fmac_f32 v81, v81, v81\n"
      "v_cndmask_b32 v62, v62, v64, s[52:53]\n"
      "v_cndmask_b32 v50, v50, v54, s[58:59]\n"
      "v_cndmask_b32 v58, v58, v62, s[58:59]\n"
      "v_cndmask_b32 v50, v50, v58, s[60:61]\n"
      "s_mov_b64 exec, s[62:63]\n"
      "ds_write_b32 v97, v50 offset:1536\n"
      "s_mov_b64 exec, -1\n"
      "s_waitcnt lgkmcnt(3)\n"
      "v_mfma_f32_32x32x16_bf16 v[2:17], v[192:195], v[90:93], 0\n"
      "ds_read_b64_tr_b16 v[86:87], v99 offset:2048\n"
      "ds_read_b64_tr_b16 v[88:89], v99 offset:2304\n"
      "s_waitcnt vmcnt(14)\n"
      "L_hb_14048:\n"
      "s_add_u32 s43, s8, 0\n"
      "v_add_u32 v109, s43, v108\n"
      "ds_read_b128 v[42:45], v109 offset:0\n"
      "ds_read_b128 v[46:49], v109 offset:1024\n"
      "v_mul_f32 v70, v70, v78\n"
      "v_fma_f32 v79, v79, v104, v106\n"
      "v_rcp_f32 v80, v80\n"
      "v_add_f32 v81, 0.5, v81\n"
      "v_mul_f32 v71, v71, v79\n"
      "global_load_dwordx4 v[160:163], v108, s[12:13] offset:0 sc1\n"
      "global_load_dwordx4 v[164:167], v108, s[12:13] offset:1024 sc1\n"
      "s_waitcnt lgkmcnt(5)\n"
      "v_mfma_f32_32x32x16_bf16 v[2:17], v[196:199], v[82:85], v[2:17]\n"
      "ds_read_b64_tr_b16 v[90:91], v99 offset:3072\n"
      "ds_read_b64_tr_b16 v[92:93], v99 offset:3328\n"
      "v_add_u32 v99, 0xfffe2000, v99\n"
      "s_waitcnt lgkmcnt(3)\n"
      "v_max3_u32 v111, v42, v43, v44\n"
      "s_waitcnt lgkmcnt(2)\n"
      "v_max3_u32 v110, v46, v47, v48\n"
      "v_max3_u32 v111, v111, v45, v49\n"
      "ds_read_b128 v[42:45], v109 offset:2048\n"
      "ds_read_b128 v[46:49], v109 offset:3072\n"
      "v_fma_f32 v80, v80, v104, v106\n"
      "v_rcp_f32 v81, v81\n"
      "v_mul_f32 v72, v72, v80\n"
      "v_fma_f32 v81, v81, v104, v106\n"
      "ds_write_b128 v95, v[26:29] offset:57344\n"
      "ds_write_b128 v95, v[30:33] offset:58368\n"
      "global_load_dwordx4 v[168:171], v108, s[12:13] offset:2048 sc1\n"
      "v_mfma_f32_32x32x16_bf16 v[2:17], v[200:203], v[86:89], v[2:17]\n"
      "ds_read_b64_tr_b16 v[82:83], v100 offset:0\n"
      "ds_read_b64_tr_b16 v[84:85], v100 offset:256\n"
      "s_waitcnt lgkmcnt(5)\n"
      "v_max3_u32 v111, v111, v42, v43\n"
      "v_max3_u32 v110, v110, v44, v45\n"
      "s_waitcnt lgkmcnt(4)\n"
      "v_max3_u32 v111, v111, v46, v47\n"
      "v_max3_u32 v110, v110, v48, v49\n"
      "v_max_u32 v111, v111, v110\n"
      "v_cmp_eq_u32 vcc, -1, v111\n"
      "s_nop 0\n"
      "s_cbranch_vccnz L_slow_14050\n"
      "L_ha_14049:\n"
      "v_mul_f32 v73, v73, v81\n"
      "v_cvt_pk_bf16_f32 v76, v70, v71\n"
      "v_cvt_pk_bf16_f32 v77, v72, v73\n"
      "s_add_u32 s20, s46, 0\n"
      "s_addc_u32 s21, s47, 0\n"
      "s_cmp_eq_u32 s33, 7\n"
      "s_cbranch_scc1 L_skip_16051\n"
      "global_store_dwordx4 v108, v[74:77], s[20:21] sc1\n"
      "L_skip_16051:\n"
      "s_cmp_eq_u32 s33, 6\n"
      "s_cbranch_scc0 L_skip_16052\n"
      "s_mov_b32 exec_hi, 0\n"
      "ds_write_b32 v94, v26 offset:1568\n"
      "s_mov_b64 exec, -1\n"
      "L_skip_16052:\n"
      "s_cmp_eq_u32 s33, 7\n"
      "s_cbranch_scc0 L_skip_16053\n"
      "s_mov_b32 exec_hi, 0\n"
      "ds_write_b32 v94, v18 offset:1572\n"
      "s_mov_b64 exec, -1\n"
      "L_skip_16053:\n"
      "global_load_dwordx4 v[172:175], v108, s[12:13] offset:3072 sc1\n"
      "s_add_u32 s12, s12, 0x1000\n"
      "s_addc_u32 s13, s13, 0\n"
      "v_mfma_f32_32x32x16_bf16 v[2:17], v[204:207], v[90:93], v[2:17]\n"
      "ds_read_b64_tr_b16 v[86:87], v100 offset:1024\n"
      "ds_read_b64_tr_b16 v[88:89], v100 offset:1280\n"
      "s_waitcnt lgkmcnt(2)\n"
      "s_barrier\n"
      "ds_read_b128 v[42:45], v94 offset:1536\n"
      "ds_read_b64 v[46:47], v94 offset:1568\n"
      "ds_read_b128 v[66:69], v96 offset:57344\n"
      "ds_read_b128 v[70:73], v96 offset:58368\n"
      "global_load_dwordx4 v[176:179], v108, s[12:13] offset:0 sc1\n"
      "v_mfma_f32_32x32x16_bf16 v[2:17], v[208:211], v[82:85], v[2:17]\n"
      "ds_read_b64_tr_b16 v[90:91], v100 offset:2048\n"
      "ds_read_b64_tr_b16 v[92:93], v100 offset:2304\n"
      "s_waitcnt lgkmcnt(5)\n"
      "v_add_f32 v42, v42, v43\n"
      "v_add_f32 v44, v44, v45\n"
      "s_waitcnt lgkmcnt(4)\n"
      "v_add_f32 v46, v46, v47\n"
      "v_add_f32 v42, v42, v44\n"
      "v_mul_f32 v46, s15, v46\n"
      "v_fma_f32 v42, v42, s15, v98\n"
      "s_waitcnt lgkmcnt(3)\n"
      "v_add_f32 v66, v18, v66\n"
      "v_fma_f32 v42, -v46, v46, v42\n"
      "global_load_dwordx4 v[180:183], v108, s[12:13] offset:1024 sc1\n"
      "v_mfma_f32_32x32x16_bf16 v[2:17], v[212:215], v[86:89], v[2:17]\n"
      "ds_read_b64_tr_b16 v[82:83], v100 offset:3072\n"
      "ds_read_b64_tr_b16 v[84:85], v100 offset:3328\n"
      "v_add_u32 v100, 0xfffe2000, v100\n"
      "v_add_f32 v67, v19, v67\n"
      "v_cmp_u_f32 vcc, v42, v42\n"
      "s_nop 0\n"
      "s_cbranch_vccnz L_slow_16055\n"
      "L_nn_16054:\n"
      "v_max_f32 v42, v42, v98\n"
      "v_add_f32 v68, v20, v68\n"
      "v_sqrt_f32 v42, v42\n"
      "v_add_f32 v69, v21, v69\n"
      "v_cvt_pk_bf16_f32 v38, -v46, -v46\n"
      "s_waitcnt lgkmcnt(4)\n"
      "v_add_f32 v70, v22, v70\n"
      "v_cvt_pk_bf16_f32 v39, v42, 0\n"
      "global_load_dwordx4 v[184:187], v108, s[12:13] offset:2048 sc1\n"
      "s_waitcnt lgkmcnt(2)\n"
      "v_mfma_f32_32x32x16_bf16 v[2:17], v[216:219], v[90:93], v[2:17]\n"
      "ds_read_b64_tr_b16 v[86:87], v101 offset:0\n"
      "ds_read_b64_tr_b16 v[88:89], v101 offset:256\n"
      "v_add_f32 v71, v23, v71\n"
      "v_lshlrev_b32 v42, 16, v39\n"
      "v_add_f32 v72, v24, v72\n"
      "v_rcp_f32 v106, v42\n"
      "v_add_f32 v73, v25, v73\n"
      "v_mul_f32 v105, 0x3fb8aa3b, v106\n"
      "s_nop 0\n"
      "v_mfma_f32_32x32x16_bf16 v[66:81], v[34:37], v[38:41], v[66:81]\n"
      "v_mul_f32 v104, -0.5, v106\n"
      "global_load_dwordx4 v[188:191], v108, s[12:13] offset:3072 sc1\n"
      "s_add_u32 s12, s12, 0x1000\n"
      "s_addc_u32 s13, s13, 0\n"
      "s_waitcnt lgkmcnt(2)\n"
      "v_mfma_f32_32x32x16_bf16 v[2:17], v[220:223], v[82:85], v[2:17]\n"
      "ds_read_b64_tr_b16 v[90:91], v101 offset:1024\n"
      "ds_read_b64_tr_b16 v[92:93], v101 offset:1280\n"
      "s_add_u32 m0, s8, 0x16000\n"
      "s_nop 0\n"
      "global_load_lds_dwordx4 v108, s[10:11] offset:0 sc1\n"
      "s_waitcnt lgkmcnt(2)\n"
      "v_mfma_f32_32x32x16_bf16 v[2:17], v[224:227], v[86:89], v[2:17]\n"
      "ds_read_b64_tr_b16 v[82:83], v101 offset:2048\n"
      "ds_read_b64_tr_b16 v[84:85], v101 offset:2304\n"
      "v_fma_f32 v74, v66, v105, -1.0\n"
      "v_exp_f32 v74, v74\n"
      "v_fma_f32 v75, v67, v105, -1.0\n"
      "v_fmac_f32 v74, v74, v74\n"
      "v_exp_f32 v75, v75\n"
      "v_fma_f32 v76, v68, v105, -1.0\n"
      "s_cmp_eq_u32 s36, 2\n"
      "s_cbranch_scc0 L_skip_18056\n"
      "s_lshl_b32 s43, s37, 5\n"
      "v_bfe_u32 v111, v0, 1, 5\n"
      "v_add_u32 v111, s43, v111\n"
      "v_and_b32 v110, 1, v0\n"
      "s_add_u32 s43, s56, 0x1e000\n"
      "v_lshlrev_b32 v109, 6, v111\n"
      "v_lshl_add_u32 v109, v110, 4, v109\n"
      "v_add_u32 v109, s43, v109\n"
      "ds_read_b128 v[46:49], v109\n"
      "v_lshlrev_b32 v110, 10, v110\n"
      "v_lshl_add_u32 v111, v111, 1, v110\n"
      "s_waitcnt lgkmcnt(0)\n"
      "ds_write_b16 v111, v46 offset:53248\n"
      "ds_write_b16_d16_hi v111, v46 offset:53376\n"
      "ds_write_b16 v111, v47 offset:53504\n"
      "ds_write_b16_d16_hi v111, v47 offset:53632\n"
      "ds_write_b16 v111, v48 offset:53760\n"
      "ds_write_b16_d16_hi v111, v48 offset:53888\n"
      "ds_write_b16 v111, v49 offset:54016\n"
      "ds_write_b16_d16_hi v111, v49 offset:54144\n"
      "L_skip_18056:\n"
      "s_waitcnt lgkmcnt(2)\n"
      "v_mfma_f32_32x32x16_bf16 v[2:17], v[228:231], v[90:93], v[2:17]\n"
      "ds_read_b64_tr_b16 v[86:87], v101 offset:3072\n"
      "ds_read_b64_tr_b16 v[88:89], v101 offset:3328\n"
      "v_add_u32 v101, 0xfffe2000, v101\n"
      "v_add_f32 v74, 0.5, v74\n"
      "v_fmac_f32 v75, v75, v75\n"
      "v_exp_f32 v76, v76\n"
      "v_fma_f32 v77, v69, v105, -1.0\n"
      "v_rcp_f32 v74, v74\n"
      "v_add_f32 v75, 0.5, v75\n"
      "s_add_u32 m0, s8, 0x16000\n"
      "s_nop 0\n"
      "global_load_lds_dwordx4 v108, s[10:11] offset:1024 sc1\n"
      "s_waitcnt lgkmcnt(2)\n"
      "v_mfma_f32_32x32x16_bf16 v[2:17], v[232:235], v[82:85], v[2:17]\n"
      "ds_read_b64_tr_b16 v[90:91], v102 offset:0\n"
      "ds_read_b64_tr_b16 v[92:93], v102 offset:256\n"
      "v_fmac_f32 v76, v76, v76\n"
      "v_exp_f32 v77, v77\n"
      "v_fma_f32 v74, v74, v104, v106\n"
      "v_rcp_f32 v75, v75\n"
      "v_add_f32 v76, 0.5, v76\n"
      "v_fmac_f32 v77, v77, v77\n"
      "s_waitcnt lgkmcnt(2)\n"
      "v_mfma_f32_32x32x16_bf16 v[2:17], v[236:239], v[86:89], v[2:17]\n"
      "ds_read_b64_tr_b16 v[82:83], v102 offset:1024\n"
      "ds_read_b64_tr_b16 v[84:85], v102 offset:1280\n"
      "v_mul_f32 v66, v66, v74\n"
      "v_fma_f32 v75, v75, v104, v106\n"
      "v_rcp_f32 v76, v76\n"
      "v_add_f32 v77, 0.5, v77\n"
      "v_mul_f32 v67, v67, v75\n"
      "v_fma_f32 v76, v76, v104, v106\n"
      "s_add_u32 m0, s8, 0x16000\n"
      "s_nop 0\n"
      "global_load_lds_dwordx4 v108, s[10:11] offset:2048 sc1\n"
      "s_waitcnt lgkmcnt(2)\n"
      "v_mfma_f32_32x32x16_bf16 v[2:17], v[240:243], v[90:93], v[2:17]\n"
      "ds_read_b64_tr_b16 v[86:87], v102 offset:2048\n"
      "ds_read_b64_tr_b16 v[88:89], v102 offset:2304\n"
      "v_rcp_f32 v77, v77\n"
      "v_mul_f32 v68, v68, v76\n"
      "v_fma_f32 v77, v77, v104, v106\n"
      "v_mul_f32 v69, v69, v77\n"
      "v_cvt_pk_bf16_f32 v74, v66, v67\n"
      "v_cvt_pk_bf16_f32 v75, v68, v69\n"
      "s_waitcnt lgkmcnt(2)\n"
      "v_mfma_f32_32x32x16_bf16 v[2:17], v[244:247], v[82:85], v[2:17]\n"
      "ds_read_b64_tr_b16 v[90:91], v102 offset:3072\n"
      "ds_read_b64_tr_b16 v[92:93], v102 offset:3328\n"
      "v_add_u32 v102, 0xfffe2000, v102\n"
      "v_fma_f32 v78, v70, v105, -1.0\n"
      "v_exp_f32 v78, v78\n"
      "v_fma_f32 v79, v71, v105, -1.0\n"
      "v_fmac_f32 v78, v78, v78\n"
      "v_exp_f32 v79, v79\n"
      "v_fma_f32 v80, v72, v105, -1.0\n"
      "s_add_u32 m0, s8, 0x16000\n"
      "s_nop 0\n"
      "global_load_lds_dwordx4 v108, s[10:11] offset:3072 sc1\n"
      "s_waitcnt lgkmcnt(2)\n"
      "v_mfma_f32_32x32x16_bf16 v[2:17], v[248:251], v[86:89], v[2:17]\n"
      "ds_read_b64_tr_b16 v[82:83], v99 offset:0\n"
      "ds_read_b64_tr_b16 v[84:85], v99 offset:256\n"
      "v_add_f32 v78, 0.5, v78\n"
      "v_fmac_f32 v79, v79, v79\n"
      "v_exp_f32 v80, v80\n"
      "v_fma_f32 v81, v73, v105, -1.0\n"
      "v_rcp_f32 v78, v78\n"
      "v_add_f32 v79, 0.5, v79\n"
      "s_waitcnt lgkmcnt(2)\n"
      "v_mfma_f32_32x32x16_bf16 v[2:17], v[252:255], v[90:93], v[2:17]\n"
      "ds_read_b64_tr_b16 v[86:87], v99 offset:1024\n"
      "ds_read_b64_tr_b16 v[88:89], v99 offset:1280\n"
      "v_fmac_f32 v80, v80, v80\n"
      "v_exp_f32 v81, v81\n"
      "v_fma_f32 v78, v78, v104, v106\n"
      "v_rcp_f32 v79, v79\n"
      "v_add_f32 v80, 0.5, v80\n"
      "v_fmac_f32 v81, v81, v81\n"
      "s_waitcnt lgkmcnt(2)\n"
      "v_mfma_f32_32x32x16_bf16 v[18:33], v[192:195], v[82:85], 0\n"
      "ds_read_b64_tr_b16 v[90:91], v99 offset:2048\n"
      "ds_read_b64_tr_b16 v[92:93], v99 offset:2304\n"
      "v_mul_f32 v70, v70, v78\n"
      "v_fma_f32 v79, v79, v104, v106\n"
      "v_rcp_f32 v80, v80\n"
      "s_waitcnt lgkmcnt(2)\n"
      "v_mfma_f32_32x32x16_bf16 v[18:33], v[196:199], v[86:89], v[18:33]\n"
      "ds_read_b64_tr_b16 v[82:83], v99 offset:3072\n"
      "ds_read_b64_tr_b16 v[84:85], v99 offset:3328\n"
      "v_add_f32 v81, 0.5, v81\n"
      "v_mul_f32 v71, v71, v79\n"
      "v_fma_f32 v80, v80, v104, v106\n"
      "ds_write_b128 v95, v[10:13] offset:32768\n"
      "s_waitcnt lgkmcnt(3)\n"
      "v_mfma_f32_32x32x16_bf16 v[18:33], v[200:203], v[90:93], v[18:33]\n"
      "ds_read_b64_tr_b16 v[86:87], v100 offset:0\n"
      "ds_read_b64_tr_b16 v[88:89], v100 offset:256\n"
      "v_rcp_f32 v81, v81\n"
      "v_mul_f32 v72, v72, v80\n"
      "ds_write_b128 v95, v[14:17] offset:33792\n"
      "s_waitcnt lgkmcnt(4)\n"
      "v_mfma_f32_32x32x16_bf16 v[18:33], v[204:207], v[82:85], v[18:33]\n"
      "ds_read_b64_tr_b16 v[90:91], v100 offset:1024\n"
      "ds_read_b64_tr_b16 v[92:93], v100 offset:1280\n"
      "v_fma_f32 v81, v81, v104, v106\n"
      "v_mul_f32 v73, v73, v81\n"
      "v_cvt_pk_bf16_f32 v76, v70, v71\n"
      "s_cmp_eq_u32 s33, 6\n"
      "s_cbranch_scc0 L_skip_18057\n"
      "s_mov_b32 exec_hi, 0\n"
      "ds_write_b32 v94, v10 offset:32\n"
      "s_mov_b64 exec, -1\n"
      "L_skip_18057:\n"
      "s_cmp_eq_u32 s33, 7\n"
      "s_cbranch_scc0 L_skip_18058\n"
      "s_mov_b32 exec_hi, 0\n"
      "ds_write_b32 v94, v2 offset:36\n"
      "s_mov_b64 exec, -1\n"
      "L_skip_18058:\n"
      "s_waitcnt lgkmcnt(3)\n"
      "v_mfma_f32_32x32x16_bf16 v[18:33], v[208:211], v[86:89], v[18:33]\n"
      "v_cvt_pk_bf16_f32 v77, v72, v73\n"
      "s_add_u32 s20, s46, 0x1c000\n"
      "s_addc_u32 s21, s47, 0\n"
      "s_cmp_eq_u32 s33, 7\n"
      "s_cbranch_scc1 L_skip_18059\n"
      "global_store_dwordx4 v108, v[74:77], s[20:21] sc1\n"
      "L_skip_18059:\n"
      "v_mfma_f32_32x32x16_bf16 v[50:65], v[86:89], v[86:89], 0\n"
      "ds_read_b64_tr_b16 v[82:83], v100 offset:2048\n"
      "ds_read_b64_tr_b16 v[84:85], v100 offset:2304\n"
      "s_waitcnt lgkmcnt(2)\n"
      "s_barrier\n"
      "ds_read_b128 v[42:45], v94 offset:0\n"
      "ds_read_b64 v[46:47], v94 offset:32\n"
      "ds_read_b128 v[66:69], v96 offset:32768\n"
      "ds_read_b128 v[70:73], v96 offset:33792\n"
      "v_mfma_f32_32x32x16_bf16 v[18:33], v[212:215], v[90:93], v[18:33]\n"
      "s_waitcnt lgkmcnt(3)\n"
      "v_add_f32 v42, v42, v43\n"
      "v_add_f32 v44, v44, v45\n"
      "s_waitcnt lgkmcnt(2)\n"
      "v_add_f32 v46, v46, v47\n"
      "v_add_f32 v42, v42, v44\n"
      "v_mul_f32 v46, s15, v46\n"
      "v_mfma_f32_32x32x16_bf16 v[50:65], v[90:93], v[90:93], v[50:65]\n"
      "ds_read_b64_tr_b16 v[86:87], v100 offset:3072\n"
      "ds_read_b64_tr_b16 v[88:89], v100 offset:3328\n"
      "v_fma_f32 v42, v42, s15, v98\n"
      "s_waitcnt lgkmcnt(3)\n"
      "v_add_f32 v66, v2, v66\n"
      "v_fma_f32 v42, -v46, v46, v42\n"
      "v_add_f32 v67, v3, v67\n"
      "v_cmp_u_f32 vcc, v42, v42\n"
      "s_nop 0\n"
      "s_cbranch_vccnz L_slow_18061\n"
      "L_nn_18060:\n"
      "v_max_f32 v42, v42, v98\n"
      "v_mfma_f32_32x32x16_bf16 v[18:33], v[216:219], v[82:85], v[18:33]\n"
      "v_add_f32 v68, v4, v68\n"
      "v_sqrt_f32 v42, v42\n"
      "v_add_f32 v69, v5, v69\n"
      "v_cvt_pk_bf16_f32 v38, -v46, -v46\n"
      "s_waitcnt lgkmcnt(2)\n"
      "v_add_f32 v70, v6, v70\n"
      "global_load_dwordx4 v[208:211], v108, s[28:29] offset:0 sc1\n"
      "v_mfma_f32_32x32x16_bf16 v[50:65], v[82:85], v[82:85], v[50:65]\n"
      "ds_read_b64_tr_b16 v[90:91], v101 offset:0\n"
      "ds_read_b64_tr_b16 v[92:93], v101 offset:256\n"
      "v_cvt_pk_bf16_f32 v39, v42, 0\n"
      "v_add_f32 v71, v7, v71\n"
      "v_lshlrev_b32 v42, 16, v39\n"
      "v_add_f32 v72, v8, v72\n"
      "v_rcp_f32 v106, v42\n"
      "global_load_dwordx4 v[212:215], v108, s[28:29] offset:1024 sc1\n"
      "s_waitcnt lgkmcnt(2)\n"
      "v_mfma_f32_32x32x16_bf16 v[18:33], v[220:223], v[86:89], v[18:33]\n"
      "v_add_f32 v73, v9, v73\n"
      "v_mul_f32 v105, 0x3fb8aa3b, v106\n"
      "s_nop 0\n"
      "v_mfma_f32_32x32x16_bf16 v[66:81], v[34:37], v[38:41], v[66:81]\n"
      "v_mul_f32 v104, -0.5, v106\n"
      "v_mfma_f32_32x32x16_bf16 v[50:65], v[86:89], v[86:89], v[50:65]\n"
      "ds_read_b64_tr_b16 v[82:83], v101 offset:1024\n"
      "ds_read_b64_tr_b16 v[84:85], v101 offset:1280\n"
      "s_waitcnt lgkmcnt(2)\n"
      "v_mfma_f32_32x32x16_bf16 v[18:33], v[224:227], v[90:93], v[18:33]\n"
      "s_nop 5\n"
      "v_fma_f32 v74, v66, v105, -1.0\n"
      "v_exp_f32 v74, v74\n"
      "v_fma_f32 v75, v67, v105, -1.0\n"
      "v_fmac_f32 v74, v74, v74\n"
      "v_exp_f32 v75, v75\n"
      "v_fma_f32 v76, v68, v105, -1.0\n"
      "s_cmp_eq_u32 s36, 3\n"
      "s_cbranch_scc0 L_skip_20062\n"
      "s_lshl_b32 s43, s37, 5\n"
      "v_bfe_u32 v111, v0, 1, 5\n"
      "v_add_u32 v111, s43, v111\n"
      "v_and_b32 v110, 1, v0\n"
      "s_add_u32 s43, s56, 0\n"
      "v_lshlrev_b32 v109, 6, v111\n"
      "v_lshl_add_u32 v109, v110, 4, v109\n"
      "v_add_u32 v109, s43, v109\n"
      "ds_read_b128 v[46:49], v109\n"
      "v_lshlrev_b32 v110, 10, v110\n"
      "v_lshl_add_u32 v111, v111, 1, v110\n"
      "s_waitcnt lgkmcnt(0)\n"
      "ds_write_b16 v111, v46 offset:55296\n"
      "ds_write_b16_d16_hi v111, v46 offset:55424\n"
      "ds_write_b16 v111, v47 offset:55552\n"
      "ds_write_b16_d16_hi v111, v47 offset:55680\n"
      "ds_write_b16 v111, v48 offset:55808\n"
      "ds_write_b16_d16_hi v111, v48 offset:55936\n"
      "ds_write_b16 v111, v49 offset:56064\n"
      "ds_write_b16_d16_hi v111, v49 offset:56192\n"
      "L_skip_20062:\n"
      "v_mfma_f32_32x32x16_bf16 v[50:65], v[90:93], v[90:93], v[50:65]\n"
      "ds_read_b64_tr_b16 v[86:87], v101 offset:2048\n"
      "ds_read_b64_tr_b16 v[88:89], v101 offset:2304\n"
      "v_add_f32 v74, 0.5, v74\n"
      "v_fmac_f32 v75, v75, v75\n"
      "v_exp_f32 v76, v76\n"
      "v_fma_f32 v77, v69, v105, -1.0\n"
      "v_rcp_f32 v74, v74\n"
      "global_load_dwordx4 v[224:227], v1, s[70:71] offset:-4096 sc1\n"
      "s_waitcnt lgkmcnt(2)\n"
      "v_mfma_f32_32x32x16_bf16 v[18:33], v[228:231], v[82:85], v[18:33]\n"
      "v_add_f32 v75, 0.5, v75\n"
      "v_fmac_f32 v76, v76, v76\n"
      "v_exp_f32 v77, v77\n"
      "v_fma_f32 v74, v74, v104, v106\n"
      "v_rcp_f32 v75, v75\n"
      "v_mfma_f32_32x32x16_bf16 v[50:65], v[82:85], v[82:85], v[50:65]\n"
      "ds_read_b64_tr_b16 v[90:91], v101 offset:3072\n"
      "ds_read_b64_tr_b16 v[92:93], v101 offset:3328\n"
      "v_add_f32 v76, 0.5, v76\n"
      "v_fmac_f32 v77, v77, v77\n"
      "v_mul_f32 v66, v66, v74\n"
      "v_fma_f32 v75, v75, v104, v106\n"
      "v_rcp_f32 v76, v76\n"
      "global_load_dwordx4 v[228:231], v1, s[70:71] offset:-2048 sc1\n"
      "s_waitcnt lgkmcnt(2)\n"
      "v_mfma_f32_32x32x16_bf16 v[18:33], v[232:235], v[86:89], v[18:33]\n"
      "v_add_f32 v77, 0.5, v77\n"
      "v_mul_f32 v67, v67, v75\n"
      "v_fma_f32 v76, v76, v104, v106\n"
      "v_rcp_f32 v77, v77\n"
      "v_mul_f32 v68, v68, v76\n"
      "v_mfma_f32_32x32x16_bf16 v[50:65], v[86:89], v[86:89], v[50:65]\n"
      "ds_read_b64_tr_b16 v[82:83], v102 offset:0\n"
      "ds_read_b64_tr_b16 v[84:85], v102 offset:256\n"
      "v_fma_f32 v77, v77, v104, v106\n"
      "v_mul_f32 v69, v69, v77\n"
      "v_cvt_pk_bf16_f32 v74, v66, v67\n"
      "v_cvt_pk_bf16_f32 v75, v68, v69\n"
      "v_fma_f32 v78, v70, v105, -1.0\n"
      "global_load_dwordx4 v[232:235], v1, s[70:71] offset:0 sc1\n"
      "s_waitcnt lgkmcnt(2)\n"
      "v_mfma_f32_32x32x16_bf16 v[18:33], v[236:239], v[90:93], v[18:33]\n"
      "v_exp_f32 v78, v78\n"
      "v_fma_f32 v79, v71, v105, -1.0\n"
      "v_fmac_f32 v78, v78, v78\n"
      "v_exp_f32 v79, v79\n"
      "v_fma_f32 v80, v72, v105, -1.0\n"
      "v_mfma_f32_32x32x16_bf16 v[50:65], v[90:93], v[90:93], v[50:65]\n"
      "ds_read_b64_tr_b16 v[86:87], v102 offset:1024\n"
      "ds_read_b64_tr_b16 v[88:89], v102 offset:1280\n"
      "v_add_f32 v78, 0.5, v78\n"
      "v_fmac_f32 v79, v79, v79\n"
      "v_exp_f32 v80, v80\n"
      "v_fma_f32 v81, v73, v105, -1.0\n"
      "v_rcp_f32 v78, v78\n"
      "global_load_dwordx4 v[236:239], v1, s[70:71] offset:2048 sc1\n"
      "s_waitcnt lgkmcnt(2)\n"
      "v_mfma_f32_32x32x16_bf16 v[18:33], v[240:243], v[82:85], v[18:33]\n"
      "ds_read_b64_tr_b16 v[90:91], v102 offset:2048\n"
      "ds_read_b64_tr_b16 v[92:93], v102 offset:2304\n"
      "v_add_f32 v79, 0.5, v79\n"
      "v_fmac_f32 v80, v80, v80\n"
      "v_exp_f32 v81, v81\n"
      "v_fma_f32 v78, v78, v104, v106\n"
      "v_rcp_f32 v79, v79\n"
      "s_cmp_eq_u32 s33, 7\n"
      "s_cbranch_scc0 L_w_20063\n"
      "s_waitcnt vmcnt(6)\n"
      "L_w_20063:\n"
      "s_waitcnt vmcnt(7)\n"
      "s_add_u32 s43, s8, 0x16000\n"
      "v_add_u32 v109, s43, v108\n"
      "ds_read_b128 v[192:195], v109 offset:0\n"
      "ds_read_b128 v[196:199], v109 offset:1024\n"
      "ds_read_b128 v[200:203], v109 offset:2048\n"
      "ds_read_b128 v[204:207], v109 offset:3072\n"
      "s_lshr_b32 s43, s8, 1\n"
      "s_add_u32 s43, s43, 0x12000\n"
      "v_add_u32 v109, s43, v108\n"
      "ds_read_b128 v[216:219], v109 offset:0\n"
      "ds_read_b128 v[220:223], v109 offset:1024\n"
      "global_load_dwordx4 v[240:243], v103, s[70:71] offset:-4096 sc1\n"
      "s_waitcnt lgkmcnt(8)\n"
      "v_mfma_f32_32x32x16_bf16 v[18:33], v[244:247], v[86:89], v[18:33]\n"
      "ds_read_b64_tr_b16 v[82:83], v102 offset:3072\n"
      "ds_read_b64_tr_b16 v[84:85], v102 offset:3328\n"
      "v_add_f32 v80, 0.5, v80\n"
      "v_fmac_f32 v81, v81, v81\n"
      "v_mul_f32 v70, v70, v78\n"
      "v_fma_f32 v79, v79, v104, v106\n"
      "v_rcp_f32 v80, v80\n"
      "v_cndmask_b32 v50, v50, v51, s[50:51]\n"
      "v_cndmask_b32 v52, v52, v53, s[50:51]\n"
      "v_cndmask_b32 v54, v54, v55, s[50:51]\n"
      "v_cndmask_b32 v56, v56, v57, s[50:51]\n"
      "v_cndmask_b32 v58, v58, v59, s[50:51]\n"
      "v_cndmask_b32 v60, v60, v61, s[50:51]\n"
      "global_load_dwordx4 v[244:247], v103, s[70:71] offset:-2048 sc1\n"
      "s_waitcnt lgkmcnt(8)\n"
      "v_mfma_f32_32x32x16_bf16 v[18:33], v[248:251], v[90:93], v[18:33]\n"
      "v_add_f32 v81, 0.5, v81\n"
      "v_mul_f32 v71, v71, v79\n"
      "v_fma_f32 v80, v80, v104, v106\n"
      "v_rcp_f32 v81, v81\n"
      "v_mul_f32 v72, v72, v80\n"
      "v_cndmask_b32 v62, v62, v63, s[50:51]\n"
      "v_cndmask_b32 v64, v64, v65, s[50:51]\n"
      "v_cndmask_b32 v50, v50, v52, s[52:53]\n"
      "v_cndmask_b32 v54, v54, v56, s[52:53]\n"
      "v_cndmask_b32 v58, v58, v60, s[52:53]\n"
      "global_load_dwordx4 v[248:251], v103, s[70:71] offset:0 sc1\n"
      "s_waitcnt lgkmcnt(0)\n"
      "v_mfma_f32_32x32x16_bf16 v[18:33], v[252:255], v[82:85], v[18:33]\n"
      "v_fma_f32 v81, v81, v104, v106\n"
      "v_mul_f32 v73, v73, v81\n"
      "v_cvt_pk_bf16_f32 v76, v70, v71\n"
      "v_cvt_pk_bf16_f32 v77, v72, v73\n"
      "s_add_u32 s20, s46, 0x38000\n"
      "s_addc_u32 s21, s47, 0\n"
      "s_cmp_eq_u32 s33, 7\n"
      "s_cbranch_scc1 L_skip_20065\n"
      "global_store_dwordx4 v108, v[74:77], s[20:21] sc1\n"
      "L_skip_20065:\n"
      "v_cndmask_b32 v62, v62, v64, s[52:53]\n"
      "v_cndmask_b32 v50, v50, v54, s[58:59]\n"
      "v_cndmask_b32 v58, v58, v62, s[58:59]\n"
      "v_cndmask_b32 v50, v50, v58, s[60:61]\n"
      "s_mov_b64 exec, s[62:63]\n"
      "ds_write_b32 v97, v50 offset:1536\n"
      "s_mov_b64 exec, -1\n"
      "L_end_14045:\n"
      "s_add_u32 s70, s70, 0x1c000\n"
      "s_addc_u32 s71, s71, 0\n"
      "s_waitcnt vmcnt(6)\n"
      "v_mfma_f32_16x16x32_bf16 v[2:5], v[112:115], v[224:227], 0\n"
      "v_and_b32 v111, 15, v0\n"
      "v_mul_u32_u24 v111, 0x108, v111\n"
      "v_bfe_u32 v110, v0, 4, 1\n"
      "v_mfma_f32_16x16x32_bf16 v[6:9], v[116:119], v[224:227], 0\n"
      "v_lshl_add_u32 v111, v110, 7, v111\n"
      "v_bfe_u32 v110, v0, 5, 1\n"
      "v_lshl_add_u32 v111, v110, 4, v111\n"
      "v_mfma_f32_16x16x32_bf16 v[10:13], v[120:123], v[224:227], 0\n"
      "ds_write_b128 v95, v[26:29] offset:57344\n"
      "v_add_u32 v102, s75, v111\n"
      "v_lshrrev_b32 v111, 5, v0\n"
      "v_mfma_f32_16x16x32_bf16 v[14:17], v[124:127], v[224:227], 0\n"
      "global_load_dwordx4 v[224:227], v1, s[70:71] offset:-4096 sc1\n"
      "ds_write_b128 v95, v[30:33] offset:58368\n"
      "v_and_b32 v110, 31, v0\n"
      "v_mul_u32_u24 v101, 0x108, v111\n"
      "v_lshl_add_u32 v101, v110, 3, v101\n"
      "s_waitcnt vmcnt(6)\n"
      "v_mfma_f32_16x16x32_bf16 v[2:5], v[128:131], v[228:231], v[2:5]\n"
      "s_cmp_eq_u32 s33, 6\n"
      "s_cbranch_scc0 L_skip_20066\n"
      "s_mov_b32 exec_hi, 0\n"
      "ds_write_b32 v94, v26 offset:1568\n"
      "s_mov_b64 exec, -1\n"
      "L_skip_20066:\n"
      "s_cmp_eq_u32 s33, 7\n"
      "s_cbranch_scc0 L_skip_20067\n"
      "s_mov_b32 exec_hi, 0\n"
      "ds_write_b32 v94, v18 offset:1572\n"
      "s_mov_b64 exec, -1\n"
      "L_skip_20067:\n"
      "v_lshlrev_b32 v100, 7, v111\n"
      "v_lshl_add_u32 v100, v110, 2, v100\n"
      "v_lshlrev_b32 v99, 10, v111\n"
      "v_mfma_f32_16x16x32_bf16 v[6:9], v[132:135], v[228:231], v[6:9]\n"
      "v_lshl_add_u32 v99, v110, 2, v99\n"
      "s_add_u32 s24, s16, s55\n"
      "s_addc_u32 s25, s17, 0\n"
      "v_mfma_f32_16x16x32_bf16 v[10:13], v[136:139], v[228:231], v[10:13]\n"
      "s_waitcnt lgkmcnt(0)\n"
      "s_barrier\n"
      "ds_read_b128 v[42:45], v94 offset:1536\n"
      "ds_read_b64 v[46:47], v94 offset:1568\n"
      "ds_read_b128 v[66:69], v96 offset:57344\n"
      "ds_read_b128 v[70:73], v96 offset:58368\n"
      "v_mfma_f32_16x16x32_bf16 v[14:17], v[140:143], v[228:231], v[14:17]\n"
      "global_load_dwordx4 v[228:231], v1, s[70:71] offset:-2048 sc1\n"
      "s_waitcnt vmcnt(6)\n"
      "v_mfma_f32_16x16x32_bf16 v[2:5], v[144:147], v[232:235], v[2:5]\n"
      "s_waitcnt lgkmcnt(3)\n"
      "v_add_f32 v42, v42, v43\n"
      "v_add_f32 v44, v44, v45\n"
      "s_waitcnt lgkmcnt(2)\n"
      "v_add_f32 v46, v46, v47\n"
      "v_add_f32 v42, v42, v44\n"
      "v_mul_f32 v46, s15, v46\n"
      "v_fma_f32 v42, v42, s15, v98\n"
      "v_mfma_f32_16x16x32_bf16 v[6:9], v[148:151], v[232:235], v[6:9]\n"
      "s_waitcnt lgkmcnt(1)\n"
      "v_add_f32 v66, v18, v66\n"
      "v_fma_f32 v42, -v46, v46, v42\n"
      "v_add_f32 v67, v19, v67\n"
      "v_max_f32 v42, v42, v98\n"
      "v_add_f32 v68, v20, v68\n"
      "v_sqrt_f32 v42, v42\n"
      "v_mfma_f32_16x16x32_bf16 v[10:13], v[152:155], v[232:235], v[10:13]\n"
      "v_add_f32 v69, v21, v69\n"
      "v_cvt_pk_bf16_f32 v38, -v46, -v46\n"
      "s_waitcnt lgkmcnt(0)\n"
      "v_add_f32 v70, v22, v70\n"
      "v_cvt_pk_bf16_f32 v39, v42, 0\n"
      "v_add_f32 v71, v23, v71\n"
      "v_lshlrev_b32 v42, 16, v39\n"
      "v_mfma_f32_16x16x32_bf16 v[14:17], v[156:159], v[232:235], v[14:17]\n"
      "global_load_dwordx4 v[232:235], v1, s[70:71] offset:0 sc1\n"
      "v_add_f32 v72, v24, v72\n"
      "v_rcp_f32 v106, v42\n"
      "v_add_f32 v73, v25, v73\n"
      "v_mul_f32 v105, 0x3fb8aa3b, v106\n"
      "s_nop 0\n"
      "v_mfma_f32_32x32x16_bf16 v[66:81], v[34:37], v[38:41], v[66:81]\n"
      "v_mul_f32 v104, -0.5, v106\n"
      "s_waitcnt vmcnt(6)\n"
      "v_mfma_f32_16x16x32_bf16 v[2:5], v[160:163], v[236:239], v[2:5]\n"
      "v_mfma_f32_16x16x32_bf16 v[6:9], v[164:167], v[236:239], v[6:9]\n"
      "v_mfma_f32_16x16x32_bf16 v[10:13], v[168:171], v[236:239], v[10:13]\n"
      "v_mfma_f32_16x16x32_bf16 v[14:17], v[172:175], v[236:239], v[14:17]\n"
      "global_load_dwordx4 v[236:239], v1, s[70:71] offset:2048 sc1\n"
      "s_nop 4\n"
      "v_fma_f32 v74, v66, v105, -1.0\n"
      "v_exp_f32 v74, v74\n"
      "v_fma_f32 v75, v67, v105, -1.0\n"
      "v_fmac_f32 v74, v74, v74\n"
      "v_exp_f32 v75, v75\n"
      "s_waitcnt vmcnt(6)\n"
      "v_mfma_f32_16x16x32_bf16 v[2:5], v[176:179], v[240:243], v[2:5]\n"
      "v_fma_f32 v76, v68, v105, -1.0\n"
      "v_add_f32 v74, 0.5, v74\n"
      "v_fmac_f32 v75, v75, v75\n"
      "v_exp_f32 v76, v76\n"
      "v_fma_f32 v77, v69, v105, -1.0\n"
      "v_mfma_f32_16x16x32_bf16 v[6:9], v[180:183], v[240:243], v[6:9]\n"
      "v_rcp_f32 v74, v74\n"
      "v_add_f32 v75, 0.5, v75\n"
      "v_fmac_f32 v76, v76, v76\n"
      "v_exp_f32 v77, v77\n"
      "v_fma_f32 v74, v74, v104, v106\n"
      "v_mfma_f32_16x16x32_bf16 v[10:13], v[184:187], v[240:243], v[10:13]\n"
      "v_rcp_f32 v75, v75\n"
      "v_add_f32 v76, 0.5, v76\n"
      "v_fmac_f32 v77, v77, v77\n"
      "v_mul_f32 v66, v66, v74\n"
      "v_mfma_f32_16x16x32_bf16 v[14:17], v[188:191], v[240:243], v[14:17]\n"
      "global_load_dwordx4 v[240:243], v103, s[70:71] offset:-4096 sc1\n"
      "v_fma_f32 v75, v75, v104, v106\n"
      "v_rcp_f32 v76, v76\n"
      "v_add_f32 v77, 0.5, v77\n"
      "v_mul_f32 v67, v67, v75\n"
      "v_fma_f32 v76, v76, v104, v106\n"
      "s_waitcnt vmcnt(6)\n"
      "v_mfma_f32_16x16x32_bf16 v[2:5], v[192:195], v[244:247], v[2:5]\n"
      "v_rcp_f32 v77, v77\n"
      "v_mul_f32 v68, v68, v76\n"
      "v_fma_f32 v77, v77, v104, v106\n"
      "v_mul_f32 v69, v69, v77\n"
      "v_cvt_pk_bf16_f32 v74, v66, v67\n"
      "v_mfma_f32_16x16x32_bf16 v[6:9], v[196:199], v[244:247], v[6:9]\n"
      "v_cvt_pk_bf16_f32 v75, v68, v69\n"
      "v_fma_f32 v78, v70, v105, -1.0\n"
      "v_exp_f32 v78, v78\n"
      "v_fma_f32 v79, v71, v105, -1.0\n"
      "v_mfma_f32_16x16x32_bf16 v[10:13], v[200:203], v[244:247], v[10:13]\n"
      "v_fmac_f32 v78, v78, v78\n"
      "v_exp_f32 v79, v79\n"
      "v_fma_f32 v80, v72, v105, -1.0\n"
      "v_add_f32 v78, 0.5, v78\n"
      "v_fmac_f32 v79, v79, v79\n"
      "v_mfma_f32_16x16x32_bf16 v[14:17], v[204:207], v[244:247], v[14:17]\n"
      "global_load_dwordx4 v[244:247], v103, s[70:71] offset:-2048 sc1\n"
      "v_exp_f32 v80, v80\n"
      "v_fma_f32 v81, v73, v105, -1.0\n"
      "v_rcp_f32 v78, v78\n"
      "v_add_f32 v79, 0.5, v79\n"
      "v_fmac_f32 v80, v80, v80\n"
      "s_waitcnt vmcnt(6)\n"
      "v_mfma_f32_16x16x32_bf16 v[2:5], v[208:211], v[248:251], v[2:5]\n"
      "v_exp_f32 v81, v81\n"
      "v_fma_f32 v78, v78, v104, v106\n"
      "v_rcp_f32 v79, v79\n"
      "v_add_f32 v80, 0.5, v80\n"
      "v_mfma_f32_16x16x32_bf16 v[6:9], v[212:215], v[248:251], v[6:9]\n"
      "v_fmac_f32 v81, v81, v81\n"
      "v_mul_f32 v70, v70, v78\n"
      "v_fma_f32 v79, v79, v104, v106\n"
      "v_rcp_f32 v80, v80\n"
      "v_add_f32 v81, 0.5, v81\n"
      "v_mfma_f32_16x16x32_bf16 v[10:13], v[216:219], v[248:251], v[10:13]\n"
      "v_mul_f32 v71, v71, v79\n"
      "v_fma_f32 v80, v80, v104, v106\n"
      "v_rcp_f32 v81, v81\n"
      "v_mul_f32 v72, v72, v80\n"
      "v_fma_f32 v81, v81, v104, v106\n"
      "v_mfma_f32_16x16x32_bf16 v[14:17], v[220:223], v[248:251], v[14:17]\n"
      "global_load_dwordx4 v[248:251], v103, s[70:71] offset:0 sc1\n"
      "v_mul_f32 v73, v73, v81\n"
      "v_cvt_pk_bf16_f32 v76, v70, v71\n"
      "v_cvt_pk_bf16_f32 v77, v72, v73\n"
      "s_add_u32 s20, s46, 0x54000\n"
      "s_addc_u32 s21, s47, 0\n"
      "s_cmp_eq_u32 s33, 7\n"
      "s_cbranch_scc1 L_skip_20068\n"
      "global_store_dwordx4 v108, v[74:77], s[20:21] sc1\n"
      "L_skip_20068:\n"
      "s_nop 3\n"
      "v_cmp_u_f32 vcc, v2, v2\n"
      "s_nop 0\n"
      "s_cbranch_vccnz L_slow_20070\n"
      "L_na_20069:\n"
      "s_add_u32 s70, s70, 0x1c000\n"
      "s_addc_u32 s71, s71, 0\n"
      "s_waitcnt vmcnt(6)\n"
      "v_mfma_f32_16x16x32_bf16 v[18:21], v[112:115], v[224:227], 0\n"
      "v_add_u32 v109, 0, v102\n"
      "v_mfma_f32_16x16x32_bf16 v[22:25], v[116:119], v[224:227], 0\n"
      "ds_write2_b32 v109, v2, v6 offset0:0 offset1:1\n"
      "v_mfma_f32_16x16x32_bf16 v[26:29], v[120:123], v[224:227], 0\n"
      "ds_write2_b32 v109, v10, v14 offset0:2 offset1:3\n"
      "v_mfma_f32_16x16x32_bf16 v[30:33], v[124:127], v[224:227], 0\n"
      "global_load_dwordx4 v[224:227], v1, s[70:71] offset:-4096 sc1\n"
      "ds_write2_b32 v109, v3, v7 offset0:8 offset1:9\n"
      "s_waitcnt vmcnt(6)\n"
      "v_mfma_f32_16x16x32_bf16 v[18:21], v[128:131], v[228:231], v[18:21]\n"
      "ds_write2_b32 v109, v11, v15 offset0:10 offset1:11\n"
      "v_mfma_f32_16x16x32_bf16 v[22:25], v[132:135], v[228:231], v[22:25]\n"
      "ds_write2_b32 v109, v4, v8 offset0:16 offset1:17\n"
      "v_mfma_f32_16x16x32_bf16 v[26:29], v[136:139], v[228:231], v[26:29]\n"
      "ds_write2_b32 v109, v12, v16 offset0:18 offset1:19\n"
      "v_mfma_f32_16x16x32_bf16 v[30:33], v[140:143], v[228:231], v[30:33]\n"
      "global_load_dwordx4 v[228:231], v1, s[70:71] offset:-2048 sc1\n"
      "ds_write2_b32 v109, v5, v9 offset0:24 offset1:25\n"
      "s_waitcnt vmcnt(6)\n"
      "v_mfma_f32_16x16x32_bf16 v[18:21], v[144:147], v[232:235], v[18:21]\n"
      "ds_write2_b32 v109, v13, v17 offset0:26 offset1:27\n"
      "v_mfma_f32_16x16x32_bf16 v[22:25], v[148:151], v[232:235], v[22:25]\n"
      "v_mfma_f32_16x16x32_bf16 v[26:29], v[152:155], v[232:235], v[26:29]\n"
      "v_mfma_f32_16x16x32_bf16 v[30:33], v[156:159], v[232:235], v[30:33]\n"
      "global_load_dwordx4 v[232:235], v1, s[70:71] offset:0 sc1\n"
      "s_waitcnt vmcnt(6)\n"
      "v_mfma_f32_16x16x32_bf16 v[18:21], v[160:163], v[236:239], v[18:21]\n"
      "s_waitcnt lgkmcnt(0)\n"
      "s_barrier\n"
      "v_mfma_f32_16x16x32_bf16 v[22:25], v[164:167], v[236:239], v[22:25]\n"
      "v_add_u32 v110, 0, v101\n"
      "ds_read_b64 v[50:51], v110 offset:0\n"
      "ds_read_b64 v[52:53], v110 offset:4224\n"
      "v_mfma_f32_16x16x32_bf16 v[26:29], v[168:171], v[236:239], v[26:29]\n"
      "ds_read_b64 v[54:55], v110 offset:8448\n"
      "ds_read_b64 v[56:57], v110 offset:12672\n"
      "v_mfma_f32_16x16x32_bf16 v[30:33], v[172:175], v[236:239], v[30:33]\n"
      "global_load_dwordx4 v[236:239], v1, s[70:71] offset:2048 sc1\n"
      "ds_read_b64 v[58:59], v110 offset:16896\n"
      "ds_read_b64 v[60:61], v110 offset:21120\n"
      "ds_read_b64 v[62:63], v110 offset:25344\n"
      "s_waitcnt vmcnt(6)\n"
      "v_mfma_f32_16x16x32_bf16 v[18:21], v[176:179], v[240:243], v[18:21]\n"
      "ds_read_b64 v[64:65], v110 offset:29568\n"
      "ds_read_b32 v97, v100 offset:49152\n"
      "v_mfma_f32_16x16x32_bf16 v[22:25], v[180:183], v[240:243], v[22:25]\n"
      "v_mfma_f32_16x16x32_bf16 v[26:29], v[184:187], v[240:243], v[26:29]\n"
      "s_waitcnt lgkmcnt(7)\n"
      "v_add_f32 v50, v50, v52\n"
      "v_add_f32 v51, v51, v53\n"
      "s_waitcnt lgkmcnt(5)\n"
      "v_add_f32 v54, v54, v56\n"
      "v_mfma_f32_16x16x32_bf16 v[30:33], v[188:191], v[240:243], v[30:33]\n"
      "global_load_dwordx4 v[240:243], v103, s[70:71] offset:-4096 sc1\n"
      "v_add_f32 v55, v55, v57\n"
      "s_waitcnt lgkmcnt(3)\n"
      "v_add_f32 v58, v58, v60\n"
      "s_waitcnt vmcnt(6)\n"
      "v_mfma_f32_16x16x32_bf16 v[18:21], v[192:195], v[244:247], v[18:21]\n"
      "v_add_f32 v59, v59, v61\n"
      "s_waitcnt lgkmcnt(1)\n"
      "v_add_f32 v62, v62, v64\n"
      "v_mfma_f32_16x16x32_bf16 v[22:25], v[196:199], v[244:247], v[22:25]\n"
      "v_add_f32 v63, v63, v65\n"
      "v_add_f32 v50, v50, v54\n"
      "v_mfma_f32_16x16x32_bf16 v[26:29], v[200:203], v[244:247], v[26:29]\n"
      "v_add_f32 v51, v51, v55\n"
      "v_add_f32 v58, v58, v62\n"
      "v_mfma_f32_16x16x32_bf16 v[30:33], v[204:207], v[244:247], v[30:33]\n"
      "global_load_dwordx4 v[244:247], v103, s[70:71] offset:-2048 sc1\n"
      "v_add_f32 v59, v59, v63\n"
      "v_add_f32 v50, v50, v58\n"
      "s_waitcnt vmcnt(6)\n"
      "v_mfma_f32_16x16x32_bf16 v[18:21], v[208:211], v[248:251], v[18:21]\n"
      "v_add_f32 v51, v51, v59\n"
      "s_waitcnt lgkmcnt(0)\n"
      "v_lshlrev_b32 v52, 16, v97\n"
      "v_mfma_f32_16x16x32_bf16 v[22:25], v[212:215], v[248:251], v[22:25]\n"
      "v_and_b32 v53, 0xffff0000, v97\n"
      "v_add_f32 v50, v50, v52\n"
      "v_mfma_f32_16x16x32_bf16 v[26:29], v[216:219], v[248:251], v[26:29]\n"
      "v_add_f32 v51, v51, v53\n"
      "v_cvt_pk_bf16_f32 v50, v50, v51\n"
      "v_mfma_f32_16x16x32_bf16 v[30:33], v[220:223], v[248:251], v[30:33]\n"
      "global_load_dwordx4 v[248:251], v103, s[70:71] offset:0 sc1\n"
      "global_store_dword v99, v50, s[24:25] sc1\n"
      "s_add_u32 s24, s24, 0x8000\n"
      "s_addc_u32 s25, s25, 0\n"
      "s_nop 3\n"
      "v_cmp_u_f32 vcc, v18, v18\n"
      "s_nop 0\n"
      "s_cbranch_vccnz L_slow_22072\n"
      "L_na_22071:\n"
      "s_add_u32 s70, s70, 0x1c000\n"
      "s_addc_u32 s71, s71, 0\n"
      "s_waitcnt vmcnt(7)\n"
      "v_mfma_f32_16x16x32_bf16 v[2:5], v[112:115], v[224:227], 0\n"
      "v_add_u32 v109, 0xe000, v102\n"
      "v_mfma_f32_16x16x32_bf16 v[6:9], v[116:119], v[224:227], 0\n"
      "ds_write2_b32 v109, v18, v22 offset0:0 offset1:1\n"
      "v_mfma_f32_16x16x32_bf16 v[10:13], v[120:123], v[224:227], 0\n"
      "ds_write2_b32 v109, v26, v30 offset0:2 offset1:3\n"
      "v_mfma_f32_16x16x32_bf16 v[14:17], v[124:127], v[224:227], 0\n"
      "global_load_dwordx4 v[224:227], v1, s[70:71] offset:-4096 sc1\n"
      "ds_write2_b32 v109, v19, v23 offset0:8 offset1:9\n"
      "s_waitcnt vmcnt(7)\n"
      "v_mfma_f32_16x16x32_bf16 v[2:5], v[128:131], v[228:231], v[2:5]\n"
      "ds_write2_b32 v109, v27, v31 offset0:10 offset1:11\n"
      "v_mfma_f32_16x16x32_bf16 v[6:9], v[132:135], v[228:231], v[6:9]\n"
      "ds_write2_b32 v109, v20, v24 offset0:16 offset1:17\n"
      "v_mfma_f32_16x16x32_bf16 v[10:13], v[136:139], v[228:231], v[10:13]\n"
      "ds_write2_b32 v109, v28, v32 offset0:18 offset1:19\n"
      "v_mfma_f32_16x16x32_bf16 v[14:17], v[140:143], v[228:231], v[14:17]\n"
      "global_load_dwordx4 v[228:231], v1, s[70:71] offset:-2048 sc1\n"
      "ds_write2_b32 v109, v21, v25 offset0:24 offset1:25\n"
      "s_waitcnt vmcnt(7)\n"
      "v_mfma_f32_16x16x32_bf16 v[2:5], v[144:147], v[232:235], v[2:5]\n"
      "ds_write2_b32 v109, v29, v33 offset0:26 offset1:27\n"
      "v_mfma_f32_16x16x32_bf16 v[6:9], v[148:151], v[232:235], v[6:9]\n"
      "v_mfma_f32_16x16x32_bf16 v[10:13], v[152:155], v[232:235], v[10:13]\n"
      "v_mfma_f32_16x16x32_bf16 v[14:17], v[156:159], v[232:235], v[14:17]\n"
      "global_load_dwordx4 v[232:235], v1, s[70:71] offset:0 sc1\n"
      "s_waitcnt vmcnt(7)\n"
      "v_mfma_f32_16x16x32_bf16 v[2:5], v[160:163], v[236:239], v[2:5]\n"
      "s_waitcnt lgkmcnt(0)\n"
      "s_barrier\n"
      "v_mfma_f32_16x16x32_bf16 v[6:9], v[164:167], v[236:239], v[6:9]\n"
      "v_add_u32 v110, 0xe000, v101\n"
      "ds_read_b64 v[50:51], v110 offset:0\n"
      "ds_read_b64 v[52:53], v110 offset:4224\n"
      "v_mfma_f32_16x16x32_bf16 v[10:13], v[168:171], v[236:239], v[10:13]\n"
      "ds_read_b64 v[54:55], v110 offset:8448\n"
      "ds_read_b64 v[56:57], v110 offset:12672\n"
      "v_mfma_f32_16x16x32_bf16 v[14:17], v[172:175], v[236:239], v[14:17]\n"
      "global_load_dwordx4 v[236:239], v1, s[70:71] offset:2048 sc1\n"
      "ds_read_b64 v[58:59], v110 offset:16896\n"
      "ds_read_b64 v[60:61], v110 offset:21120\n"
      "ds_read_b64 v[62:63], v110 offset:25344\n"
      "s_waitcnt vmcnt(7)\n"
      "v_mfma_f32_16x16x32_bf16 v[2:5], v[176:179], v[240:243], v[2:5]\n"
      "ds_read_b64 v[64:65], v110 offset:29568\n"
      "ds_read_b32 v97, v100 offset:51200\n"
      "v_mfma_f32_16x16x32_bf16 v[6:9], v[180:183], v[240:243], v[6:9]\n"
      "v_mfma_f32_16x16x32_bf16 v[10:13], v[184:187], v[240:243], v[10:13]\n"
      "s_waitcnt lgkmcnt(7)\n"
      "v_add_f32 v50, v50, v52\n"
      "v_add_f32 v51, v51, v53\n"
      "s_waitcnt lgkmcnt(5)\n"
      "v_add_f32 v54, v54, v56\n"
      "v_mfma_f32_16x16x32_bf16 v[14:17], v[188:191], v[240:243], v[14:17]\n"
      "global_load_dwordx4 v[240:243], v103, s[70:71] offset:-4096 sc1\n"
      "v_add_f32 v55, v55, v57\n"
      "s_waitcnt lgkmcnt(3)\n"
      "v_add_f32 v58, v58, v60\n"
      "s_waitcnt vmcnt(7)\n"
      "v_mfma_f32_16x16x32_bf16 v[2:5], v[192:195], v[244:247], v[2:5]\n"
      "v_add_f32 v59, v59, v61\n"
      "s_waitcnt lgkmcnt(1)\n"
      "v_add_f32 v62, v62, v64\n"
      "v_mfma_f32_16x16x32_bf16 v[6:9], v[196:199], v[244:247], v[6:9]\n"
      "v_add_f32 v63, v63, v65\n"
      "v_add_f32 v50, v50, v54\n"
      "v_mfma_f32_16x16x32_bf16 v[10:13], v[200:203], v[244:247], v[10:13]\n"
      "v_add_f32 v51, v51, v55\n"
      "v_add_f32 v58, v58, v62\n"
      "v_mfma_f32_16x16x32_bf16 v[14:17], v[204:207], v[244:247], v[14:17]\n"
      "global_load_dwordx4 v[244:247], v103, s[70:71] offset:-2048 sc1\n"
      "v_add_f32 v59, v59, v63\n"
      "v_add_f32 v50, v50, v58\n"
      "s_waitcnt vmcnt(7)\n"
      "v_mfma_f32_16x16x32_bf16 v[2:5], v[208:211], v[248:251], v[2:5]\n"
      "v_add_f32 v51, v51, v59\n"
      "s_waitcnt lgkmcnt(0)\n"
      "v_lshlrev_b32 v52, 16, v97\n"
      "v_mfma_f32_16x16x32_bf16 v[6:9], v[212:215], v[248:251], v[6:9]\n"
      "v_and_b32 v53, 0xffff0000, v97\n"
      "v_add_f32 v50, v50, v52\n"
      "v_mfma_f32_16x16x32_bf16 v[10:13], v[216:219], v[248:251], v[10:13]\n"
      "v_add_f32 v51, v51, v53\n"
      "v_cvt_pk_bf16_f32 v50, v50, v51\n"
      "v_mfma_f32_16x16x32_bf16 v[14:17], v[220:223], v[248:251], v[14:17]\n"
      "global_load_dwordx4 v[248:251], v103, s[70:71] offset:0 sc1\n"
      "global_store_dword v99, v50, s[24:25] sc1\n"
      "s_add_u32 s24, s24, 0x8000\n"
      "s_addc_u32 s25, s25, 0\n"
      "s_nop 3\n"
      "v_cmp_u_f32 vcc, v2, v2\n"
      "s_nop 0\n"
      "s_cbranch_vccnz L_slow_24074\n"
      "L_na_24073:\n"
      "s_waitcnt vmcnt(7)\n"
      "v_mfma_f32_16x16x32_bf16 v[18:21], v[112:115], v[224:227], 0\n"
      "v_add_u32 v109, 0x16400, v102\n"
      "v_mfma_f32_16x16x32_bf16 v[22:25], v[116:119], v[224:227], 0\n"
      "ds_write2_b32 v109, v2, v6 offset0:0 offset1:1\n"
      "v_mfma_f32_16x16x32_bf16 v[26:29], v[120:123], v[224:227], 0\n"
      "ds_write2_b32 v109, v10, v14 offset0:2 offset1:3\n"
      "v_mfma_f32_16x16x32_bf16 v[30:33], v[124:127], v[224:227], 0\n"
      "ds_write2_b32 v109, v3, v7 offset0:8 offset1:9\n"
      "s_waitcnt vmcnt(6)\n"
      "v_mfma_f32_16x16x32_bf16 v[18:21], v[128:131], v[228:231], v[18:21]\n"
      "ds_write2_b32 v109, v11, v15 offset0:10 offset1:11\n"
      "v_mfma_f32_16x16x32_bf16 v[22:25], v[132:135], v[228:231], v[22:25]\n"
      "ds_write2_b32 v109, v4, v8 offset0:16 offset1:17\n"
      "v_mfma_f32_16x16x32_bf16 v[26:29], v[136:139], v[228:231], v[26:29]\n"
      "ds_write2_b32 v109, v12, v16 offset0:18 offset1:19\n"
      "v_mfma_f32_16x16x32_bf16 v[30:33], v[140:143], v[228:231], v[30:33]\n"
      "ds_write2_b32 v109, v5, v9 offset0:24 offset1:25\n"
      "s_waitcnt vmcnt(5)\n"
      "v_mfma_f32_16x16x32_bf16 v[18:21], v[144:147], v[232:235], v[18:21]\n"
      "ds_write2_b32 v109, v13, v17 offset0:26 offset1:27\n"
      "v_mfma_f32_16x16x32_bf16 v[22:25], v[148:151], v[232:235], v[22:25]\n"
      "v_mfma_f32_16x16x32_bf16 v[26:29], v[152:155], v[232:235], v[26:29]\n"
      "v_mfma_f32_16x16x32_bf16 v[30:33], v[156:159], v[232:235], v[30:33]\n"
      "s_waitcnt vmcnt(4)\n"
      "v_mfma_f32_16x16x32_bf16 v[18:21], v[160:163], v[236:239], v[18:21]\n"
      "s_waitcnt lgkmcnt(0)\n"
      "s_barrier\n"
      "v_mfma_f32_16x16x32_bf16 v[22:25], v[164:167], v[236:239], v[22:25]\n"
      "v_add_u32 v110, 0x16400, v101\n"
      "ds_read_b64 v[50:51], v110 offset:0\n"
      "ds_read_b64 v[52:53], v110 offset:4224\n"
      "v_mfma_f32_16x16x32_bf16 v[26:29], v[168:171], v[236:239], v[26:29]\n"
      "ds_read_b64 v[54:55], v110 offset:8448\n"
      "ds_read_b64 v[56:57], v110 offset:12672\n"
      "v_mfma_f32_16x16x32_bf16 v[30:33], v[172:175], v[236:239], v[30:33]\n"
      "ds_read_b64 v[58:59], v110 offset:16896\n"
      "ds_read_b64 v[60:61], v110 offset:21120\n"
      "ds_read_b64 v[62:63], v110 offset:25344\n"
      "s_waitcnt vmcnt(3)\n"
      "v_mfma_f32_16x16x32_bf16 v[18:21], v[176:179], v[240:243], v[18:21]\n"
      "ds_read_b64 v[64:65], v110 offset:29568\n"
      "ds_read_b32 v97, v100 offset:53248\n"
      "v_mfma_f32_16x16x32_bf16 v[22:25], v[180:183], v[240:243], v[22:25]\n"
      "v_mfma_f32_16x16x32_bf16 v[26:29], v[184:187], v[240:243], v[26:29]\n"
      "s_waitcnt lgkmcnt(7)\n"
      "v_add_f32 v50, v50, v52\n"
      "v_add_f32 v51, v51, v53\n"
      "s_waitcnt lgkmcnt(5)\n"
      "v_add_f32 v54, v54, v56\n"
      "v_mfma_f32_16x16x32_bf16 v[30:33], v[188:191], v[240:243], v[30:33]\n"
      "v_add_f32 v55, v55, v57\n"
      "s_waitcnt lgkmcnt(3)\n"
      "v_add_f32 v58, v58, v60\n"
      "s_waitcnt vmcnt(2)\n"
      "v_mfma_f32_16x16x32_bf16 v[18:21], v[192:195], v[244:247], v[18:21]\n"
      "v_add_f32 v59, v59, v61\n"
      "s_waitcnt lgkmcnt(1)\n"
      "v_add_f32 v62, v62, v64\n"
      "v_mfma_f32_16x16x32_bf16 v[22:25], v[196:199], v[244:247], v[22:25]\n"
      "v_add_f32 v63, v63, v65\n"
      "v_add_f32 v50, v50, v54\n"
      "v_mfma_f32_16x16x32_bf16 v[26:29], v[200:203], v[244:247], v[26:29]\n"
      "v_add_f32 v51, v51, v55\n"
      "v_add_f32 v58, v58, v62\n"
      "v_mfma_f32_16x16x32_bf16 v[30:33], v[204:207], v[244:247], v[30:33]\n"
      "v_add_f32 v59, v59, v63\n"
      "v_add_f32 v50, v50, v58\n"
      "s_waitcnt vmcnt(1)\n"
      "v_mfma_f32_16x16x32_bf16 v[18:21], v[208:211], v[248:251], v[18:21]\n"
      "v_add_f32 v51, v51, v59\n"
      "s_waitcnt lgkmcnt(0)\n"
      "v_lshlrev_b32 v52, 16, v97\n"
      "v_mfma_f32_16x16x32_bf16 v[22:25], v[212:215], v[248:251], v[22:25]\n"
      "v_and_b32 v53, 0xffff0000, v97\n"
      "v_add_f32 v50, v50, v52\n"
      "v_mfma_f32_16x16x32_bf16 v[26:29], v[216:219], v[248:251], v[26:29]\n"
      "v_add_f32 v51, v51, v53\n"
      "v_cvt_pk_bf16_f32 v50, v50, v51\n"
      "v_mfma_f32_16x16x32_bf16 v[30:33], v[220:223], v[248:251], v[30:33]\n"
      "global_store_dword v99, v50, s[24:25] sc1\n"
      "s_add_u32 s24, s24, 0x8000\n"
      "s_addc_u32 s25, s25, 0\n"
      "s_nop 3\n"
      "v_cmp_u_f32 vcc, v18, v18\n"
      "s_nop 0\n"
      "s_cbranch_vccnz L_slow_26076\n"
      "L_na_26075:\n"
      "v_add_u32 v109, 0x1e800, v102\n"
      "ds_write2_b32 v109, v18, v22 offset0:0 offset1:1\n"
      "ds_write2_b32 v109, v26, v30 offset0:2 offset1:3\n"
      "ds_write2_b32 v109, v19, v23 offset0:8 offset1:9\n"
      "ds_write2_b32 v109, v27, v31 offset0:10 offset1:11\n"
      "ds_write2_b32 v109, v20, v24 offset0:16 offset1:17\n"
      "ds_write2_b32 v109, v28, v32 offset0:18 offset1:19\n"
      "ds_write2_b32 v109, v21, v25 offset0:24 offset1:25\n"
      "ds_write2_b32 v109, v29, v33 offset0:26 offset1:27\n"
      "s_waitcnt lgkmcnt(0)\n"
      "s_barrier\n"
      "v_add_u32 v110, 0x1e800, v101\n"
      "ds_read_b64 v[50:51], v110 offset:0\n"
      "ds_read_b64 v[52:53], v110 offset:4224\n"
      "ds_read_b64 v[54:55], v110 offset:8448\n"
      "ds_read_b64 v[56:57], v110 offset:12672\n"
      "ds_read_b64 v[58:59], v110 offset:16896\n"
      "ds_read_b64 v[60:61], v110 offset:21120\n"
      "ds_read_b64 v[62:63], v110 offset:25344\n"
      "ds_read_b64 v[64:65], v110 offset:29568\n"
      "ds_read_b32 v97, v100 offset:55296\n"
      "s_waitcnt lgkmcnt(7)\n"
      "v_add_f32 v50, v50, v52\n"
      "v_add_f32 v51, v51, v53\n"
      "s_waitcnt lgkmcnt(5)\n"
      "v_add_f32 v54, v54, v56\n"
      "v_add_f32 v55, v55, v57\n"
      "s_waitcnt lgkmcnt(3)\n"
      "v_add_f32 v58, v58, v60\n"
      "v_add_f32 v59, v59, v61\n"
      "s_waitcnt lgkmcnt(1)\n"
      "v_add_f32 v62, v62, v64\n"
      "v_add_f32 v63, v63, v65\n"
      "v_add_f32 v50, v50, v54\n"
      "v_add_f32 v51, v51, v55\n"
      "v_add_f32 v58, v58, v62\n"
      "v_add_f32 v59, v59, v63\n"
      "v_add_f32 v50, v50, v58\n"
      "v_add_f32 v51, v51, v59\n"
      "s_waitcnt lgkmcnt(0)\n"
      "v_lshlrev_b32 v52, 16, v97\n"
      "v_and_b32 v53, 0xffff0000, v97\n"
      "v_add_f32 v50, v50, v52\n"
      "v_add_f32 v51, v51, v53\n"
      "v_cvt_pk_bf16_f32 v50, v50, v51\n"
      "global_store_dword v99, v50, s[24:25] sc1\n"
      "s_add_u32 s24, s24, 0x8000\n"
      "s_addc_u32 s25, s25, 0\n"
      "s_endpgm\n"
      "L_slow_19:\n"
      "s_cmp_le_i32 s41, 0\n"
      "s_cbranch_scc1 L_ua_18\n"
      "s_sub_u32 s41, s41, 1\n"
      "global_load_dwordx4 v[152:155], v63, s[30:31] sc1\n"
      "s_waitcnt vmcnt(0) lgkmcnt(0)\n"
      "s_nop 15\n"
      "s_branch L_ub_17\n"
      "L_slow_2023:\n"
      "L_sr_3024:\n"
      "s_cmp_le_i32 s41, 0\n"
      "s_cbranch_scc1 L_sa_2022\n"
      "s_sub_u32 s41, s41, 1\n"
      "s_mov_b32 m0, s44\n"
      "s_nop 0\n"
      "global_load_lds_dwordx4 v108, s[24:25] offset:0 sc1\n"
      "s_add_u32 m0, s44, 0x1c00\n"
      "s_nop 0\n"
      "global_load_lds_dwordx4 v108, s[24:25] offset:1024 sc1\n"
      "s_waitcnt vmcnt(0)\n"
      "ds_read_b128 v[38:41], v91 offset:0\n"
      "ds_read_b128 v[42:45], v91 offset:16\n"
      "ds_read_b128 v[46:49], v91 offset:32\n"
      "ds_read_b128 v[50:53], v91 offset:48\n"
      "s_waitcnt lgkmcnt(3)\n"
      "v_xor_b32 v109, s38, v39\n"
      "v_bitop3_b32 v109, v109, v41, s38 bitop3:0xf6\n"
      "s_waitcnt lgkmcnt(2)\n"
      "v_bitop3_b32 v109, v109, v43, s38 bitop3:0xf6\n"
      "v_bitop3_b32 v109, v109, v45, s38 bitop3:0xf6\n"
      "s_waitcnt lgkmcnt(1)\n"
      "v_bitop3_b32 v109, v109, v47, s38 bitop3:0xf6\n"
      "v_bitop3_b32 v109, v109, v49, s38 bitop3:0xf6\n"
      "s_waitcnt lgkmcnt(0)\n"
      "v_bitop3_b32 v109, v109, v51, s38 bitop3:0xf6\n"
      "v_bitop3_b32 v109, v109, v53, s38 bitop3:0xf6\n"
      "v_cmp_ne_u32 vcc, 0, v109\n"
      "s_nop 0\n"
      "s_cbranch_vccnz L_sr_3024\n"
      "s_waitcnt vmcnt(0) lgkmcnt(0)\n"
      "s_nop 15\n"
      "s_branch L_sa_2022\n"
      "L_slow_4029:\n"
      "L_sr_5030:\n"
      "s_cmp_le_i32 s41, 0\n"
      "s_cbranch_scc1 L_sa_4026\n"
      "s_sub_u32 s41, s41, 1\n"
      "s_mov_b32 m0, s44\n"
      "s_nop 0\n"
      "global_load_lds_dwordx4 v108, s[24:25] offset:0 sc1\n"
      "s_add_u32 m0, s44, 0x1c00\n"
      "s_nop 0\n"
      "global_load_lds_dwordx4 v108, s[24:25] offset:1024 sc1\n"
      "s_waitcnt vmcnt(0)\n"
      "ds_read_b128 v[38:41], v91 offset:0\n"
      "ds_read_b128 v[42:45], v91 offset:16\n"
      "ds_read_b128 v[46:49], v91 offset:32\n"
      "ds_read_b128 v[50:53], v91 offset:48\n"
      "s_waitcnt lgkmcnt(3)\n"
      "v_xor_b32 v109, s38, v39\n"
      "v_bitop3_b32 v109, v109, v41, s38 bitop3:0xf6\n"
      "s_waitcnt lgkmcnt(2)\n"
      "v_bitop3_b32 v109, v109, v43, s38 bitop3:0xf6\n"
      "v_bitop3_b32 v109, v109, v45, s38 bitop3:0xf6\n"
      "s_waitcnt lgkmcnt(1)\n"
      "v_bitop3_b32 v109, v109, v47, s38 bitop3:0xf6\n"
      "v_bitop3_b32 v109, v109, v49, s38 bitop3:0xf6\n"
      "s_waitcnt lgkmcnt(0)\n"
      "v_bitop3_b32 v109, v109, v51, s38 bitop3:0xf6\n"
      "v_bitop3_b32 v109, v109, v53, s38 bitop3:0xf6\n"
      "v_cmp_ne_u32 vcc, 0, v109\n"
      "s_nop 0\n"
      "s_cbranch_vccnz L_sr_5030\n"
      "s_waitcnt vmcnt(0) lgkmcnt(0)\n"
      "s_nop 15\n"
      "s_branch L_sa_4026\n"
      "L_slow_6034:\n"
      "L_hr_7035:\n"
      "s_cmp_le_i32 s41, 0\n"
      "s_cbranch_scc1 L_ha_6033\n"
      "s_sub_u32 s41, s41, 1\n"
      "s_add_u32 m0, s8, 0xe000\n"
      "s_nop 0\n"
      "global_load_lds_dwordx4 v108, s[4:5] offset:0 sc1\n"
      "global_load_lds_dwordx4 v108, s[4:5] offset:1024 sc1\n"
      "global_load_lds_dwordx4 v108, s[4:5] offset:2048 sc1\n"
      "global_load_lds_dwordx4 v108, s[4:5] offset:3072 sc1\n"
      "s_waitcnt vmcnt(0)\n"
      "v_mov_b32 v111, 0\n"
      "ds_read_b128 v[42:45], v109 offset:0\n"
      "s_waitcnt lgkmcnt(0)\n"
      "v_max3_u32 v111, v111, v42, v43\n"
      "v_max3_u32 v111, v111, v44, v45\n"
      "ds_read_b128 v[42:45], v109 offset:1024\n"
      "s_waitcnt lgkmcnt(0)\n"
      "v_max3_u32 v111, v111, v42, v43\n"
      "v_max3_u32 v111, v111, v44, v45\n"
      "ds_read_b128 v[42:45], v109 offset:2048\n"
      "s_waitcnt lgkmcnt(0)\n"
      "v_max3_u32 v111, v111, v42, v43\n"
      "v_max3_u32 v111, v111, v44, v45\n"
      "ds_read_b128 v[42:45], v109 offset:3072\n"
      "s_waitcnt lgkmcnt(0)\n"
      "v_max3_u32 v111, v111, v42, v43\n"
      "v_max3_u32 v111, v111, v44, v45\n"
      "v_cmp_eq_u32 vcc, -1, v111\n"
      "s_nop 0\n"
      "s_cbranch_vccnz L_hr_7035\n"
      "s_waitcnt vmcnt(0) lgkmcnt(0)\n"
      "s_nop 15\n"
      "s_branch L_ha_6033\n"
      "L_slow_8040:\n"
      "L_hr_9041:\n"
      "s_cmp_le_i32 s41, 0\n"
      "s_cbranch_scc1 L_ha_8039\n"
      "s_sub_u32 s41, s41, 1\n"
      "s_add_u32 m0, s8, 0\n"
      "s_nop 0\n"
      "global_load_lds_dwordx4 v108, s[48:49] offset:0 sc1\n"
      "global_load_lds_dwordx4 v108, s[48:49] offset:1024 sc1\n"
      "global_load_lds_dwordx4 v108, s[48:49] offset:2048 sc1\n"
      "global_load_lds_dwordx4 v108, s[48:49] offset:3072 sc1\n"
      "s_waitcnt vmcnt(0)\n"
      "v_mov_b32 v111, 0\n"
      "ds_read_b128 v[42:45], v109 offset:0\n"
      "s_waitcnt lgkmcnt(0)\n"
      "v_max3_u32 v111, v111, v42, v43\n"
      "v_max3_u32 v111, v111, v44, v45\n"
      "ds_read_b128 v[42:45], v109 offset:1024\n"
      "s_waitcnt lgkmcnt(0)\n"
      "v_max3_u32 v111, v111, v42, v43\n"
      "v_max3_u32 v111, v111, v44, v45\n"
      "ds_read_b128 v[42:45], v109 offset:2048\n"
      "s_waitcnt lgkmcnt(0)\n"
      "v_max3_u32 v111, v111, v42, v43\n"
      "v_max3_u32 v111, v111, v44, v45\n"
      "ds_read_b128 v[42:45], v109 offset:3072\n"
      "s_waitcnt lgkmcnt(0)\n"
      "v_max3_u32 v111, v111, v42, v43\n"
      "v_max3_u32 v111, v111, v44, v45\n"
      "v_cmp_eq_u32 vcc, -1, v111\n"
      "s_nop 0\n"
      "s_cbranch_vccnz L_hr_9041\n"
      "s_waitcnt vmcnt(0) lgkmcnt(0)\n"
      "s_nop 15\n"
      "s_branch L_ha_8039\n"
      "L_slow_10042:\n"
      "s_cmp_lg_u32 s6, 0\n"
      "s_cbranch_scc1 L_nn_10041\n"
      "s_mov_b32 s6, 1\n"
      "s_sub_u32 s12, s10, 0x5000\n"
      "s_subb_u32 s13, s11, 0\n"
      "s_waitcnt vmcnt(0)\n"
      "s_waitcnt lgkmcnt(0)\n"
      "s_barrier\n"
      "L_rr_11043:\n"
      "s_add_u32 m0, s8, 0xe000\n"
      "s_nop 0\n"
      "global_load_lds_dwordx4 v108, s[4:5] offset:0 sc1\n"
      "global_load_lds_dwordx4 v108, s[4:5] offset:1024 sc1\n"
      "global_load_lds_dwordx4 v108, s[4:5] offset:2048 sc1\n"
      "global_load_lds_dwordx4 v108, s[4:5] offset:3072 sc1\n"
      "s_add_u32 s43, s8, 0xe000\n"
      "v_add_u32 v109, s43, v108\n"
      "s_waitcnt vmcnt(0)\n"
      "v_mov_b32 v111, 0\n"
      "ds_read_b128 v[42:45], v109 offset:0\n"
      "s_waitcnt lgkmcnt(0)\n"
      "v_max3_u32 v111, v111, v42, v43\n"
      "v_max3_u32 v111, v111, v44, v45\n"
      "ds_read_b128 v[42:45], v109 offset:1024\n"
      "s_waitcnt lgkmcnt(0)\n"
      "v_max3_u32 v111, v111, v42, v43\n"
      "v_max3_u32 v111, v111, v44, v45\n"
      "ds_read_b128 v[42:45], v109 offset:2048\n"
      "s_waitcnt lgkmcnt(0)\n"
      "v_max3_u32 v111, v111, v42, v43\n"
      "v_max3_u32 v111, v111, v44, v45\n"
      "ds_read_b128 v[42:45], v109 offset:3072\n"
      "s_waitcnt lgkmcnt(0)\n"
      "v_max3_u32 v111, v111, v42, v43\n"
      "v_max3_u32 v111, v111, v44, v45\n"
      "v_cmp_eq_u32 vcc, -1, v111\n"
      "s_nop 0\n"
      "s_cbranch_vccz L_rd_11044\n"
      "s_cmp_le_i32 s41, 0\n"
      "s_cbranch_scc1 L_rd_11044\n"
      "s_sub_u32 s41, s41, 1\n"
      "s_branch L_rr_11043\n"
      "L_rd_11044:\n"
      "L_rr_11045:\n"
      "s_add_u32 m0, s8, 0x16000\n"
      "s_nop 0\n"
      "global_load_lds_dwordx4 v108, s[30:31] offset:0 sc1\n"
      "global_load_lds_dwordx4 v108, s[30:31] offset:1024 sc1\n"
      "global_load_lds_dwordx4 v108, s[30:31] offset:2048 sc1\n"
      "global_load_lds_dwordx4 v108, s[30:31] offset:3072 sc1\n"
      "s_add_u32 s43, s8, 0x16000\n"
      "v_add_u32 v109, s43, v108\n"
      "s_waitcnt vmcnt(0)\n"
      "v_mov_b32 v111, 0\n"
      "ds_read_b128 v[42:45], v109 offset:0\n"
      "s_waitcnt lgkmcnt(0)\n"
      "v_max3_u32 v111, v111, v42, v43\n"
      "v_max3_u32 v111, v111, v44, v45\n"
      "ds_read_b128 v[42:45], v109 offset:1024\n"
      "s_waitcnt lgkmcnt(0)\n"
      "v_max3_u32 v111, v111, v42, v43\n"
      "v_max3_u32 v111, v111, v44, v45\n"
      "ds_read_b128 v[42:45], v109 offset:2048\n"
      "s_waitcnt lgkmcnt(0)\n"
      "v_max3_u32 v111, v111, v42, v43\n"
      "v_max3_u32 v111, v111, v44, v45\n"
      "ds_read_b128 v[42:45], v109 offset:3072\n"
      "s_waitcnt lgkmcnt(0)\n"
      "v_max3_u32 v111, v111, v42, v43\n"
      "v_max3_u32 v111, v111, v44, v45\n"
      "v_cmp_eq_u32 vcc, -1, v111\n"
      "s_nop 0\n"
      "s_cbranch_vccz L_rd_11046\n"
      "s_cmp_le_i32 s41, 0\n"
      "s_cbranch_scc1 L_rd_11046\n"
      "s_sub_u32 s41, s41, 1\n"
      "s_branch L_rr_11045\n"
      "L_rd_11046:\n"
      "L_rr_11047:\n"
      "s_add_u32 m0, s8, 0x1e000\n"
      "s_nop 0\n"
      "global_load_lds_dwordx4 v108, s[44:45] offset:0 sc1\n"
      "global_load_lds_dwordx4 v108, s[44:45] offset:1024 sc1\n"
      "global_load_lds_dwordx4 v108, s[44:45] offset:2048 sc1\n"
      "global_load_lds_dwordx4 v108, s[44:45] offset:3072 sc1\n"
      "s_add_u32 s43, s8, 0x1e000\n"
      "v_add_u32 v109, s43, v108\n"
      "s_waitcnt vmcnt(0)\n"
      "v_mov_b32 v111, 0\n"
      "ds_read_b128 v[42:45], v109 offset:0\n"
      "s_waitcnt lgkmcnt(0)\n"
      "v_max3_u32 v111, v111, v42, v43\n"
      "v_max3_u32 v111, v111, v44, v45\n"
      "ds_read_b128 v[42:45], v109 offset:1024\n"
      "s_waitcnt lgkmcnt(0)\n"
      "v_max3_u32 v111, v111, v42, v43\n"
      "v_max3_u32 v111, v111, v44, v45\n"
      "ds_read_b128 v[42:45], v109 offset:2048\n"
      "s_waitcnt lgkmcnt(0)\n"
      "v_max3_u32 v111, v111, v42, v43\n"
      "v_max3_u32 v111, v111, v44, v45\n"
      "ds_read_b128 v[42:45], v109 offset:3072\n"
      "s_waitcnt lgkmcnt(0)\n"
      "v_max3_u32 v111, v111, v42, v43\n"
      "v_max3_u32 v111, v111, v44, v45\n"
      "v_cmp_eq_u32 vcc, -1, v111\n"
      "s_nop 0\n"
      "s_cbranch_vccz L_rd_11048\n"
      "s_cmp_le_i32 s41, 0\n"
      "s_cbranch_scc1 L_rd_11048\n"
      "s_sub_u32 s41, s41, 1\n"
      "s_branch L_rr_11047\n"
      "L_rd_11048:\n"
      "L_rr_11049:\n"
      "s_add_u32 m0, s8, 0\n"
      "s_nop 0\n"
      "global_load_lds_dwordx4 v108, s[48:49] offset:0 sc1\n"
      "global_load_lds_dwordx4 v108, s[48:49] offset:1024 sc1\n"
      "global_load_lds_dwordx4 v108, s[48:49] offset:2048 sc1\n"
      "global_load_lds_dwordx4 v108, s[48:49] offset:3072 sc1\n"
      "s_add_u32 s43, s8, 0\n"
      "v_add_u32 v109, s43, v108\n"
      "s_waitcnt vmcnt(0)\n"
      "v_mov_b32 v111, 0\n"
      "ds_read_b128 v[42:45], v109 offset:0\n"
      "s_waitcnt lgkmcnt(0)\n"
      "v_max3_u32 v111, v111, v42, v43\n"
      "v_max3_u32 v111, v111, v44, v45\n"
      "ds_read_b128 v[42:45], v109 offset:1024\n"
      "s_waitcnt lgkmcnt(0)\n"
      "v_max3_u32 v111, v111, v42, v43\n"
      "v_max3_u32 v111, v111, v44, v45\n"
      "ds_read_b128 v[42:45], v109 offset:2048\n"
      "s_waitcnt lgkmcnt(0)\n"
      "v_max3_u32 v111, v111, v42, v43\n"
      "v_max3_u32 v111, v111, v44, v45\n"
      "ds_read_b128 v[42:45], v109 offset:3072\n"
      "s_waitcnt lgkmcnt(0)\n"
      "v_max3_u32 v111, v111, v42, v43\n"
      "v_max3_u32 v111, v111, v44, v45\n"
      "v_cmp_eq_u32 vcc, -1, v111\n"
      "s_nop 0\n"
      "s_cbranch_vccz L_rd_11050\n"
      "s_cmp_le_i32 s41, 0\n"
      "s_cbranch_scc1 L_rd_11050\n"
      "s_sub_u32 s41, s41, 1\n"
      "s_branch L_rr_11049\n"
      "L_rd_11050:\n"
      "v_bfe_u32 v111, v0, 5, 1\n"
      "v_bfe_u32 v110, v0, 2, 2\n"
      "v_lshl_add_u32 v111, v111, 3, v110\n"
      "v_lshlrev_b32 v111, 6, v111\n"
      "v_bfe_u32 v110, v0, 4, 1\n"
      "v_lshl_add_u32 v111, v110, 5, v111\n"
      "v_and_b32 v110, 3, v0\n"
      "v_lshl_add_u32 v111, v110, 3, v111\n"
      "v_add_u32 v99, s64, v111\n"
      "v_add_u32 v100, s65, v111\n"
      "v_add_u32 v101, s66, v111\n"
      "v_add_u32 v102, s67, v111\n"
      "s_barrier\n"
      "s_waitcnt vmcnt(0) lgkmcnt(0)\n"
      "s_nop 15\n"
      "s_branch L_gemm1\n"
      "L_slow_12044:\n"
      "s_cmp_lg_u32 s6, 0\n"
      "s_cbranch_scc1 L_nn_12043\n"
      "s_mov_b32 s6, 1\n"
      "s_sub_u32 s12, s10, 0x5000\n"
      "s_subb_u32 s13, s11, 0\n"
      "s_waitcnt vmcnt(0)\n"
      "s_waitcnt lgkmcnt(0)\n"
      "s_barrier\n"
      "L_rr_13045:\n"
      "s_add_u32 m0, s8, 0xe000\n"
      "s_nop 0\n"
      "global_load_lds_dwordx4 v108, s[4:5] offset:0 sc1\n"
      "global_load_lds_dwordx4 v108, s[4:5] offset:1024 sc1\n"
      "global_load_lds_dwordx4 v108, s[4:5] offset:2048 sc1\n"
      "global_load_lds_dwordx4 v108, s[4:5] offset:3072 sc1\n"
      "s_add_u32 s43, s8, 0xe000\n"
      "v_add_u32 v109, s43, v108\n"
      "s_waitcnt vmcnt(0)\n"
      "v_mov_b32 v111, 0\n"
      "ds_read_b128 v[42:45], v109 offset:0\n"
      "s_waitcnt lgkmcnt(0)\n"
      "v_max3_u32 v111, v111, v42, v43\n"
      "v_max3_u32 v111, v111, v44, v45\n"
      "ds_read_b128 v[42:45], v109 offset:1024\n"
      "s_waitcnt lgkmcnt(0)\n"
      "v_max3_u32 v111, v111, v42, v43\n"
      "v_max3_u32 v111, v111, v44, v45\n"
      "ds_read_b128 v[42:45], v109 offset:2048\n"
      "s_waitcnt lgkmcnt(0)\n"
      "v_max3_u32 v111, v111, v42, v43\n"
      "v_max3_u32 v111, v111, v44, v45\n"
      "ds_read_b128 v[42:45], v109 offset:3072\n"
      "s_waitcnt lgkmcnt(0)\n"
      "v_max3_u32 v111, v111, v42, v43\n"
      "v_max3_u32 v111, v111, v44, v45\n"
      "v_cmp_eq_u32 vcc, -1, v111\n"
      "s_nop 0\n"
      "s_cbranch_vccz L_rd_13046\n"
      "s_cmp_le_i32 s41, 0\n"
      "s_cbranch_scc1 L_rd_13046\n"
      "s_sub_u32 s41, s41, 1\n"
      "s_branch L_rr_13045\n"
      "L_rd_13046:\n"
      "L_rr_13047:\n"
      "s_add_u32 m0, s8, 0x16000\n"
      "s_nop 0\n"
      "global_load_lds_dwordx4 v108, s[30:31] offset:0 sc1\n"
      "global_load_lds_dwordx4 v108, s[30:31] offset:1024 sc1\n"
      "global_load_lds_dwordx4 v108, s[30:31] offset:2048 sc1\n"
      "global_load_lds_dwordx4 v108, s[30:31] offset:3072 sc1\n"
      "s_add_u32 s43, s8, 0x16000\n"
      "v_add_u32 v109, s43, v108\n"
      "s_waitcnt vmcnt(0)\n"
      "v_mov_b32 v111, 0\n"
      "ds_read_b128 v[42:45], v109 offset:0\n"
      "s_waitcnt lgkmcnt(0)\n"
      "v_max3_u32 v111, v111, v42, v43\n"
      "v_max3_u32 v111, v111, v44, v45\n"
      "ds_read_b128 v[42:45], v109 offset:1024\n"
      "s_waitcnt lgkmcnt(0)\n"
      "v_max3_u32 v111, v111, v42, v43\n"
      "v_max3_u32 v111, v111, v44, v45\n"
      "ds_read_b128 v[42:45], v109 offset:2048\n"
      "s_waitcnt lgkmcnt(0)\n"
      "v_max3_u32 v111, v111, v42, v43\n"
      "v_max3_u32 v111, v111, v44, v45\n"
      "ds_read_b128 v[42:45], v109 offset:3072\n"
      "s_waitcnt lgkmcnt(0)\n"
      "v_max3_u32 v111, v111, v42, v43\n"
      "v_max3_u32 v111, v111, v44, v45\n"
      "v_cmp_eq_u32 vcc, -1, v111\n"
      "s_nop 0\n"
      "s_cbranch_vccz L_rd_13048\n"
      "s_cmp_le_i32 s41, 0\n"
      "s_cbranch_scc1 L_rd_13048\n"
      "s_sub_u32 s41, s41, 1\n"
      "s_branch L_rr_13047\n"
      "L_rd_13048:\n"
      "L_rr_13049:\n"
      "s_add_u32 m0, s8, 0x1e000\n"
      "s_nop 0\n"
      "global_load_lds_dwordx4 v108, s[44:45] offset:0 sc1\n"
      "global_load_lds_dwordx4 v108, s[44:45] offset:1024 sc1\n"
      "global_load_lds_dwordx4 v108, s[44:45] offset:2048 sc1\n"
      "global_load_lds_dwordx4 v108, s[44:45] offset:3072 sc1\n"
      "s_add_u32 s43, s8, 0x1e000\n"
      "v_add_u32 v109, s43, v108\n"
      "s_waitcnt vmcnt(0)\n"
      "v_mov_b32 v111, 0\n"
      "ds_read_b128 v[42:45], v109 offset:0\n"
      "s_waitcnt lgkmcnt(0)\n"
      "v_max3_u32 v111, v111, v42, v43\n"
      "v_max3_u32 v111, v111, v44, v45\n"
      "ds_read_b128 v[42:45], v109 offset:1024\n"
      "s_waitcnt lgkmcnt(0)\n"
      "v_max3_u32 v111, v111, v42, v43\n"
      "v_max3_u32 v111, v111, v44, v45\n"
      "ds_read_b128 v[42:45], v109 offset:2048\n"
      "s_waitcnt lgkmcnt(0)\n"
      "v_max3_u32 v111, v111, v42, v43\n"
      "v_max3_u32 v111, v111, v44, v45\n"
      "ds_read_b128 v[42:45], v109 offset:3072\n"
      "s_waitcnt lgkmcnt(0)\n"
      "v_max3_u32 v111, v111, v42, v43\n"
      "v_max3_u32 v111, v111, v44, v45\n"
      "v_cmp_eq_u32 vcc, -1, v111\n"
      "s_nop 0\n"
      "s_cbranch_vccz L_rd_13050\n"
      "s_cmp_le_i32 s41, 0\n"
      "s_cbranch_scc1 L_rd_13050\n"
      "s_sub_u32 s41, s41, 1\n"
      "s_branch L_rr_13049\n"
      "L_rd_13050:\n"
      "L_rr_13051:\n"
      "s_add_u32 m0, s8, 0\n"
      "s_nop 0\n"
      "global_load_lds_dwordx4 v108, s[48:49] offset:0 sc1\n"
      "global_load_lds_dwordx4 v108, s[48:49] offset:1024 sc1\n"
      "global_load_lds_dwordx4 v108, s[48:49] offset:2048 sc1\n"
      "global_load_lds_dwordx4 v108, s[48:49] offset:3072 sc1\n"
      "s_add_u32 s43, s8, 0\n"
      "v_add_u32 v109, s43, v108\n"
      "s_waitcnt vmcnt(0)\n"
      "v_mov_b32 v111, 0\n"
      "ds_read_b128 v[42:45], v109 offset:0\n"
      "s_waitcnt lgkmcnt(0)\n"
      "v_max3_u32 v111, v111, v42, v43\n"
      "v_max3_u32 v111, v111, v44, v45\n"
      "ds_read_b128 v[42:45], v109 offset:1024\n"
      "s_waitcnt lgkmcnt(0)\n"
      "v_max3_u32 v111, v111, v42, v43\n"
      "v_max3_u32 v111, v111, v44, v45\n"
      "ds_read_b128 v[42:45], v109 offset:2048\n"
      "s_waitcnt lgkmcnt(0)\n"
      "v_max3_u32 v111, v111, v42, v43\n"
      "v_max3_u32 v111, v111, v44, v45\n"
      "ds_read_b128 v[42:45], v109 offset:3072\n"
      "s_waitcnt lgkmcnt(0)\n"
      "v_max3_u32 v111, v111, v42, v43\n"
      "v_max3_u32 v111, v111, v44, v45\n"
      "v_cmp_eq_u32 vcc, -1, v111\n"
      "s_nop 0\n"
      "s_cbranch_vccz L_rd_13052\n"
      "s_cmp_le_i32 s41, 0\n"
      "s_cbranch_scc1 L_rd_13052\n"
      "s_sub_u32 s41, s41, 1\n"
      "s_branch L_rr_13051\n"
      "L_rd_13052:\n"
      "v_bfe_u32 v111, v0, 5, 1\n"
      "v_bfe_u32 v110, v0, 2, 2\n"
      "v_lshl_add_u32 v111, v111, 3, v110\n"
      "v_lshlrev_b32 v111, 6, v111\n"
      "v_bfe_u32 v110, v0, 4, 1\n"
      "v_lshl_add_u32 v111, v110, 5, v111\n"
      "v_and_b32 v110, 3, v0\n"
      "v_lshl_add_u32 v111, v110, 3, v111\n"
      "v_add_u32 v99, s64, v111\n"
      "v_add_u32 v100, s65, v111\n"
      "v_add_u32 v101, s66, v111\n"
      "v_add_u32 v102, s67, v111\n"
      "s_barrier\n"
      "s_waitcnt vmcnt(0) lgkmcnt(0)\n"
      "s_nop 15\n"
      "s_branch L_gemm1\n"
      "L_slow_14050:\n"
      "L_hr_15051:\n"
      "s_cmp_le_i32 s41, 0\n"
      "s_cbranch_scc1 L_ha_14049\n"
      "s_sub_u32 s41, s41, 1\n"
      "s_add_u32 m0, s8, 0\n"
      "s_nop 0\n"
      "global_load_lds_dwordx4 v108, s[48:49] offset:0 sc1\n"
      "global_load_lds_dwordx4 v108, s[48:49] offset:1024 sc1\n"
      "global_load_lds_dwordx4 v108, s[48:49] offset:2048 sc1\n"
      "global_load_lds_dwordx4 v108, s[48:49] offset:3072 sc1\n"
      "s_waitcnt vmcnt(0)\n"
      "v_mov_b32 v111, 0\n"
      "ds_read_b128 v[42:45], v109 offset:0\n"
      "s_waitcnt lgkmcnt(0)\n"
      "v_max3_u32 v111, v111, v42, v43\n"
      "v_max3_u32 v111, v111, v44, v45\n"
      "ds_read_b128 v[42:45], v109 offset:1024\n"
      "s_waitcnt lgkmcnt(0)\n"
      "v_max3_u32 v111, v111, v42, v43\n"
      "v_max3_u32 v111, v111, v44, v45\n"
      "ds_read_b128 v[42:45], v109 offset:2048\n"
      "s_waitcnt lgkmcnt(0)\n"
      "v_max3_u32 v111, v111, v42, v43\n"
      "v_max3_u32 v111, v111, v44, v45\n"
      "ds_read_b128 v[42:45], v109 offset:3072\n"
      "s_waitcnt lgkmcnt(0)\n"
      "v_max3_u32 v111, v111, v42, v43\n"
      "v_max3_u32 v111, v111, v44, v45\n"
      "v_cmp_eq_u32 vcc, -1, v111\n"
      "s_nop 0\n"
      "s_cbranch_vccnz L_hr_15051\n"
      "s_waitcnt vmcnt(0) lgkmcnt(0)\n"
      "s_nop 15\n"
      "s_branch L_ha_14049\n"
      "L_slow_16055:\n"
      "s_cmp_lg_u32 s6, 0\n"
      "s_cbranch_scc1 L_nn_16054\n"
      "s_mov_b32 s6, 1\n"
      "s_sub_u32 s12, s10, 0x5000\n"
      "s_subb_u32 s13, s11, 0\n"
      "s_waitcnt vmcnt(0)\n"
      "s_waitcnt lgkmcnt(0)\n"
      "s_barrier\n"
      "L_rr_17056:\n"
      "s_add_u32 m0, s8, 0xe000\n"
      "s_nop 0\n"
      "global_load_lds_dwordx4 v108, s[4:5] offset:0 sc1\n"
      "global_load_lds_dwordx4 v108, s[4:5] offset:1024 sc1\n"
      "global_load_lds_dwordx4 v108, s[4:5] offset:2048 sc1\n"
      "global_load_lds_dwordx4 v108, s[4:5] offset:3072 sc1\n"
      "s_add_u32 s43, s8, 0xe000\n"
      "v_add_u32 v109, s43, v108\n"
      "s_waitcnt vmcnt(0)\n"
      "v_mov_b32 v111, 0\n"
      "ds_read_b128 v[42:45], v109 offset:0\n"
      "s_waitcnt lgkmcnt(0)\n"
      "v_max3_u32 v111, v111, v42, v43\n"
      "v_max3_u32 v111, v111, v44, v45\n"
      "ds_read_b128 v[42:45], v109 offset:1024\n"
      "s_waitcnt lgkmcnt(0)\n"
      "v_max3_u32 v111, v111, v42, v43\n"
      "v_max3_u32 v111, v111, v44, v45\n"
      "ds_read_b128 v[42:45], v109 offset:2048\n"
      "s_waitcnt lgkmcnt(0)\n"
      "v_max3_u32 v111, v111, v42, v43\n"
      "v_max3_u32 v111, v111, v44, v45\n"
      "ds_read_b128 v[42:45], v109 offset:3072\n"
      "s_waitcnt lgkmcnt(0)\n"
      "v_max3_u32 v111, v111, v42, v43\n"
      "v_max3_u32 v111, v111, v44, v45\n"
      "v_cmp_eq_u32 vcc, -1, v111\n"
      "s_nop 0\n"
      "s_cbranch_vccz L_rd_17057\n"
      "s_cmp_le_i32 s41, 0\n"
      "s_cbranch_scc1 L_rd_17057\n"
      "s_sub_u32 s41, s41, 1\n"
      "s_branch L_rr_17056\n"
      "L_rd_17057:\n"
      "L_rr_17058:\n"
      "s_add_u32 m0, s8, 0x16000\n"
      "s_nop 0\n"
      "global_load_lds_dwordx4 v108, s[30:31] offset:0 sc1\n"
      "global_load_lds_dwordx4 v108, s[30:31] offset:1024 sc1\n"
      "global_load_lds_dwordx4 v108, s[30:31] offset:2048 sc1\n"
      "global_load_lds_dwordx4 v108, s[30:31] offset:3072 sc1\n"
      "s_add_u32 s43, s8, 0x16000\n"
      "v_add_u32 v109, s43, v108\n"
      "s_waitcnt vmcnt(0)\n"
      "v_mov_b32 v111, 0\n"
      "ds_read_b128 v[42:45], v109 offset:0\n"
      "s_waitcnt lgkmcnt(0)\n"
      "v_max3_u32 v111, v111, v42, v43\n"
      "v_max3_u32 v111, v111, v44, v45\n"
      "ds_read_b128 v[42:45], v109 offset:1024\n"
      "s_waitcnt lgkmcnt(0)\n"
      "v_max3_u32 v111, v111, v42, v43\n"
      "v_max3_u32 v111, v111, v44, v45\n"
      "ds_read_b128 v[42:45], v109 offset:2048\n"
      "s_waitcnt lgkmcnt(0)\n"
      "v_max3_u32 v111, v111, v42, v43\n"
      "v_max3_u32 v111, v111, v44, v45\n"
      "ds_read_b128 v[42:45], v109 offset:3072\n"
      "s_waitcnt lgkmcnt(0)\n"
      "v_max3_u32 v111, v111, v42, v43\n"
      "v_max3_u32 v111, v111, v44, v45\n"
      "v_cmp_eq_u32 vcc, -1, v111\n"
      "s_nop 0\n"
      "s_cbranch_vccz L_rd_17059\n"
      "s_cmp_le_i32 s41, 0\n"
      "s_cbranch_scc1 L_rd_17059\n"
      "s_sub_u32 s41, s41, 1\n"
      "s_branch L_rr_17058\n"
      "L_rd_17059:\n"
      "L_rr_17060:\n"
      "s_add_u32 m0, s8, 0x1e000\n"
      "s_nop 0\n"
      "global_load_lds_dwordx4 v108, s[44:45] offset:0 sc1\n"
      "global_load_lds_dwordx4 v108, s[44:45] offset:1024 sc1\n"
      "global_load_lds_dwordx4 v108, s[44:45] offset:2048 sc1\n"
      "global_load_lds_dwordx4 v108, s[44:45] offset:3072 sc1\n"
      "s_add_u32 s43, s8, 0x1e000\n"
      "v_add_u32 v109, s43, v108\n"
      "s_waitcnt vmcnt(0)\n"
      "v_mov_b32 v111, 0\n"
      "ds_read_b128 v[42:45], v109 offset:0\n"
      "s_waitcnt lgkmcnt(0)\n"
      "v_max3_u32 v111, v111, v42, v43\n"
      "v_max3_u32 v111, v111, v44, v45\n"
      "ds_read_b128 v[42:45], v109 offset:1024\n"
      "s_waitcnt lgkmcnt(0)\n"
      "v_max3_u32 v111, v111, v42, v43\n"
      "v_max3_u32 v111, v111, v44, v45\n"
      "ds_read_b128 v[42:45], v109 offset:2048\n"
      "s_waitcnt lgkmcnt(0)\n"
      "v_max3_u32 v111, v111, v42, v43\n"
      "v_max3_u32 v111, v111, v44, v45\n"
      "ds_read_b128 v[42:45], v109 offset:3072\n"
      "s_waitcnt lgkmcnt(0)\n"
      "v_max3_u32 v111, v111, v42, v43\n"
      "v_max3_u32 v111, v111, v44, v45\n"
      "v_cmp_eq_u32 vcc, -1, v111\n"
      "s_nop 0\n"
      "s_cbranch_vccz L_rd_17061\n"
      "s_cmp_le_i32 s41, 0\n"
      "s_cbranch_scc1 L_rd_17061\n"
      "s_sub_u32 s41, s41, 1\n"
      "s_branch L_rr_17060\n"
      "L_rd_17061:\n"
      "L_rr_17062:\n"
      "s_add_u32 m0, s8, 0\n"
      "s_nop 0\n"
      "global_load_lds_dwordx4 v108, s[48:49] offset:0 sc1\n"
      "global_load_lds_dwordx4 v108, s[48:49] offset:1024 sc1\n"
      "global_load_lds_dwordx4 v108, s[48:49] offset:2048 sc1\n"
      "global_load_lds_dwordx4 v108, s[48:49] offset:3072 sc1\n"
      "s_add_u32 s43, s8, 0\n"
      "v_add_u32 v109, s43, v108\n"
      "s_waitcnt vmcnt(0)\n"
      "v_mov_b32 v111, 0\n"
      "ds_read_b128 v[42:45], v109 offset:0\n"
      "s_waitcnt lgkmcnt(0)\n"
      "v_max3_u32 v111, v111, v42, v43\n"
      "v_max3_u32 v111, v111, v44, v45\n"
      "ds_read_b128 v[42:45], v109 offset:1024\n"
      "s_waitcnt lgkmcnt(0)\n"
      "v_max3_u32 v111, v111, v42, v43\n"
      "v_max3_u32 v111, v111, v44, v45\n"
      "ds_read_b128 v[42:45], v109 offset:2048\n"
      "s_waitcnt lgkmcnt(0)\n"
      "v_max3_u32 v111, v111, v42, v43\n"
      "v_max3_u32 v111, v111, v44, v45\n"
      "ds_read_b128 v[42:45], v109 offset:3072\n"
      "s_waitcnt lgkmcnt(0)\n"
      "v_max3_u32 v111, v111, v42, v43\n"
      "v_max3_u32 v111, v111, v44, v45\n"
      "v_cmp_eq_u32 vcc, -1, v111\n"
      "s_nop 0\n"
      "s_cbranch_vccz L_rd_17063\n"
      "s_cmp_le_i32 s41, 0\n"
      "s_cbranch_scc1 L_rd_17063\n"
      "s_sub_u32 s41, s41, 1\n"
      "s_branch L_rr_17062\n"
      "L_rd_17063:\n"
      "v_bfe_u32 v111, v0, 5, 1\n"
      "v_bfe_u32 v110, v0, 2, 2\n"
      "v_lshl_add_u32 v111, v111, 3, v110\n"
      "v_lshlrev_b32 v111, 6, v111\n"
      "v_bfe_u32 v110, v0, 4, 1\n"
      "v_lshl_add_u32 v111, v110, 5, v111\n"
      "v_and_b32 v110, 3, v0\n"
      "v_lshl_add_u32 v111, v110, 3, v111\n"
      "v_add_u32 v99, s64, v111\n"
      "v_add_u32 v100, s65, v111\n"
      "v_add_u32 v101, s66, v111\n"
      "v_add_u32 v102, s67, v111\n"
      "s_barrier\n"
      "s_waitcnt vmcnt(0) lgkmcnt(0)\n"
      "s_nop 15\n"
      "s_branch L_gemm1\n"
      "L_slow_18061:\n"
      "s_cmp_lg_u32 s6, 0\n"
      "s_cbranch_scc1 L_nn_18060\n"
      "s_mov_b32 s6, 1\n"
      "s_sub_u32 s12, s10, 0x5000\n"
      "s_subb_u32 s13, s11, 0\n"
      "s_waitcnt vmcnt(0)\n"
      "s_waitcnt lgkmcnt(0)\n"
      "s_barrier\n"
      "L_rr_19062:\n"
      "s_add_u32 m0, s8, 0xe000\n"
      "s_nop 0\n"
      "global_load_lds_dwordx4 v108, s[4:5] offset:0 sc1\n"
      "global_load_lds_dwordx4 v108, s[4:5] offset:1024 sc1\n"
      "global_load_lds_dwordx4 v108, s[4:5] offset:2048 sc1\n"
      "global_load_lds_dwordx4 v108, s[4:5] offset:3072 sc1\n"
      "s_add_u32 s43, s8, 0xe000\n"
      "v_add_u32 v109, s43, v108\n"
      "s_waitcnt vmcnt(0)\n"
      "v_mov_b32 v111, 0\n"
      "ds_read_b128 v[42:45], v109 offset:0\n"
      "s_waitcnt lgkmcnt(0)\n"
      "v_max3_u32 v111, v111, v42, v43\n"
      "v_max3_u32 v111, v111, v44, v45\n"
      "ds_read_b128 v[42:45], v109 offset:1024\n"
      "s_waitcnt lgkmcnt(0)\n"
      "v_max3_u32 v111, v111, v42, v43\n"
      "v_max3_u32 v111, v111, v44, v45\n"
      "ds_read_b128 v[42:45], v109 offset:2048\n"
      "s_waitcnt lgkmcnt(0)\n"
      "v_max3_u32 v111, v111, v42, v43\n"
      "v_max3_u32 v111, v111, v44, v45\n"
      "ds_read_b128 v[42:45], v109 offset:3072\n"
      "s_waitcnt lgkmcnt(0)\n"
      "v_max3_u32 v111, v111, v42, v43\n"
      "v_max3_u32 v111, v111, v44, v45\n"
      "v_cmp_eq_u32 vcc, -1, v111\n"
      "s_nop 0\n"
      "s_cbranch_vccz L_rd_19063\n"
      "s_cmp_le_i32 s41, 0\n"
      "s_cbranch_scc1 L_rd_19063\n"
      "s_sub_u32 s41, s41, 1\n"
      "s_branch L_rr_19062\n"
      "L_rd_19063:\n"
      "L_rr_19064:\n"
      "s_add_u32 m0, s8, 0x16000\n"
      "s_nop 0\n"
      "global_load_lds_dwordx4 v108, s[30:31] offset:0 sc1\n"
      "global_load_lds_dwordx4 v108, s[30:31] offset:1024 sc1\n"
      "global_load_lds_dwordx4 v108, s[30:31] offset:2048 sc1\n"
      "global_load_lds_dwordx4 v108, s[30:31] offset:3072 sc1\n"
      "s_add_u32 s43, s8, 0x16000\n"
      "v_add_u32 v109, s43, v108\n"
      "s_waitcnt vmcnt(0)\n"
      "v_mov_b32 v111, 0\n"
      "ds_read_b128 v[42:45], v109 offset:0\n"
      "s_waitcnt lgkmcnt(0)\n"
      "v_max3_u32 v111, v111, v42, v43\n"
      "v_max3_u32 v111, v111, v44, v45\n"
      "ds_read_b128 v[42:45], v109 offset:1024\n"
      "s_waitcnt lgkmcnt(0)\n"
      "v_max3_u32 v111, v111, v42, v43\n"
      "v_max3_u32 v111, v111, v44, v45\n"
      "ds_read_b128 v[42:45], v109 offset:2048\n"
      "s_waitcnt lgkmcnt(0)\n"
      "v_max3_u32 v111, v111, v42, v43\n"
      "v_max3_u32 v111, v111, v44, v45\n"
      "ds_read_b128 v[42:45], v109 offset:3072\n"
      "s_waitcnt lgkmcnt(0)\n"
      "v_max3_u32 v111, v111, v42, v43\n"
      "v_max3_u32 v111, v111, v44, v45\n"
      "v_cmp_eq_u32 vcc, -1, v111\n"
      "s_nop 0\n"
      "s_cbranch_vccz L_rd_19065\n"
      "s_cmp_le_i32 s41, 0\n"
      "s_cbranch_scc1 L_rd_19065\n"
      "s_sub_u32 s41, s41, 1\n"
      "s_branch L_rr_19064\n"
      "L_rd_19065:\n"
      "L_rr_19066:\n"
      "s_add_u32 m0, s8, 0x1e000\n"
      "s_nop 0\n"
      "global_load_lds_dwordx4 v108, s[44:45] offset:0 sc1\n"
      "global_load_lds_dwordx4 v108, s[44:45] offset:1024 sc1\n"
      "global_load_lds_dwordx4 v108, s[44:45] offset:2048 sc1\n"
      "global_load_lds_dwordx4 v108, s[44:45] offset:3072 sc1\n"
      "s_add_u32 s43, s8, 0x1e000\n"
      "v_add_u32 v109, s43, v108\n"
      "s_waitcnt vmcnt(0)\n"
      "v_mov_b32 v111, 0\n"
      "ds_read_b128 v[42:45], v109 offset:0\n"
      "s_waitcnt lgkmcnt(0)\n"
      "v_max3_u32 v111, v111, v42, v43\n"
      "v_max3_u32 v111, v111, v44, v45\n"
      "ds_read_b128 v[42:45], v109 offset:1024\n"
      "s_waitcnt lgkmcnt(0)\n"
      "v_max3_u32 v111, v111, v42, v43\n"
      "v_max3_u32 v111, v111, v44, v45\n"
      "ds_read_b128 v[42:45], v109 offset:2048\n"
      "s_waitcnt lgkmcnt(0)\n"
      "v_max3_u32 v111, v111, v42, v43\n"
      "v_max3_u32 v111, v111, v44, v45\n"
      "ds_read_b128 v[42:45], v109 offset:3072\n"
      "s_waitcnt lgkmcnt(0)\n"
      "v_max3_u32 v111, v111, v42, v43\n"
      "v_max3_u32 v111, v111, v44, v45\n"
      "v_cmp_eq_u32 vcc, -1, v111\n"
      "s_nop 0\n"
      "s_cbranch_vccz L_rd_19067\n"
      "s_cmp_le_i32 s41, 0\n"
      "s_cbranch_scc1 L_rd_19067\n"
      "s_sub_u32 s41, s41, 1\n"
      "s_branch L_rr_19066\n"
      "L_rd_19067:\n"
      "L_rr_19068:\n"
      "s_add_u32 m0, s8, 0\n"
      "s_nop 0\n"
      "global_load_lds_dwordx4 v108, s[48:49] offset:0 sc1\n"
      "global_load_lds_dwordx4 v108, s[48:49] offset:1024 sc1\n"
      "global_load_lds_dwordx4 v108, s[48:49] offset:2048 sc1\n"
      "global_load_lds_dwordx4 v108, s[48:49] offset:3072 sc1\n"
      "s_add_u32 s43, s8, 0\n"
      "v_add_u32 v109, s43, v108\n"
      "s_waitcnt vmcnt(0)\n"
      "v_mov_b32 v111, 0\n"
      "ds_read_b128 v[42:45], v109 offset:0\n"
      "s_waitcnt lgkmcnt(0)\n"
      "v_max3_u32 v111, v111, v42, v43\n"
      "v_max3_u32 v111, v111, v44, v45\n"
      "ds_read_b128 v[42:45], v109 offset:1024\n"
      "s_waitcnt lgkmcnt(0)\n"
      "v_max3_u32 v111, v111, v42, v43\n"
      "v_max3_u32 v111, v111, v44, v45\n"
      "ds_read_b128 v[42:45], v109 offset:2048\n"
      "s_waitcnt lgkmcnt(0)\n"
      "v_max3_u32 v111, v111, v42, v43\n"
      "v_max3_u32 v111, v111, v44, v45\n"
      "ds_read_b128 v[42:45], v109 offset:3072\n"
      "s_waitcnt lgkmcnt(0)\n"
      "v_max3_u32 v111, v111, v42, v43\n"
      "v_max3_u32 v111, v111, v44, v45\n"
      "v_cmp_eq_u32 vcc, -1, v111\n"
      "s_nop 0\n"
      "s_cbranch_vccz L_rd_19069\n"
      "s_cmp_le_i32 s41, 0\n"
      "s_cbranch_scc1 L_rd_19069\n"
      "s_sub_u32 s41, s41, 1\n"
      "s_branch L_rr_19068\n"
      "L_rd_19069:\n"
      "v_bfe_u32 v111, v0, 5, 1\n"
      "v_bfe_u32 v110, v0, 2, 2\n"
      "v_lshl_add_u32 v111, v111, 3, v110\n"
      "v_lshlrev_b32 v111, 6, v111\n"
      "v_bfe_u32 v110, v0, 4, 1\n"
      "v_lshl_add_u32 v111, v110, 5, v111\n"
      "v_and_b32 v110, 3, v0\n"
      "v_lshl_add_u32 v111, v110, 3, v111\n"
      "v_add_u32 v99, s64, v111\n"
      "v_add_u32 v100, s65, v111\n"
      "v_add_u32 v101, s66, v111\n"
      "v_add_u32 v102, s67, v111\n"
      "s_barrier\n"
      "s_waitcnt vmcnt(0) lgkmcnt(0)\n"
      "s_nop 15\n"
      "s_branch L_gemm1\n"
      "L_slow_20070:\n"
      "s_waitcnt vmcnt(0)\n"
      "s_sub_u32 s50, s70, 0x1c000\n"
      "s_subb_u32 s51, s71, 0\n"
      "L_nr_21071:\n"
      "global_load_dwordx4 v[224:227], v1, s[50:51] offset:-4096 sc1\n"
      "global_load_dwordx4 v[228:231], v1, s[50:51] offset:-2048 sc1\n"
      "global_load_dwordx4 v[232:235], v1, s[50:51] offset:0 sc1\n"
      "global_load_dwordx4 v[236:239], v1, s[50:51] offset:2048 sc1\n"
      "global_load_dwordx4 v[240:243], v103, s[50:51] offset:-4096 sc1\n"
      "global_load_dwordx4 v[244:247], v103, s[50:51] offset:-2048 sc1\n"
      "global_load_dwordx4 v[248:251], v103, s[50:51] offset:0 sc1\n"
      "v_mov_b32 v111, 0\n"
      "s_waitcnt vmcnt(6)\n"
      "v_max3_u32 v111, v111, v224, v225\n"
      "v_max3_u32 v111, v111, v226, v227\n"
      "s_waitcnt vmcnt(5)\n"
      "v_max3_u32 v111, v111, v228, v229\n"
      "v_max3_u32 v111, v111, v230, v231\n"
      "s_waitcnt vmcnt(4)\n"
      "v_max3_u32 v111, v111, v232, v233\n"
      "v_max3_u32 v111, v111, v234, v235\n"
      "s_waitcnt vmcnt(3)\n"
      "v_max3_u32 v111, v111, v236, v237\n"
      "v_max3_u32 v111, v111, v238, v239\n"
      "s_waitcnt vmcnt(2)\n"
      "v_max3_u32 v111, v111, v240, v241\n"
      "v_max3_u32 v111, v111, v242, v243\n"
      "s_waitcnt vmcnt(1)\n"
      "v_max3_u32 v111, v111, v244, v245\n"
      "v_max3_u32 v111, v111, v246, v247\n"
      "s_waitcnt vmcnt(0)\n"
      "v_max3_u32 v111, v111, v248, v249\n"
      "v_max3_u32 v111, v111, v250, v251\n"
      "v_cmp_eq_u32 vcc, -1, v111\n"
      "s_nop 0\n"
      "s_cbranch_vccz L_nd_21072\n"
      "s_cmp_le_i32 s41, 0\n"
      "s_cbranch_scc1 L_nd_21072\n"
      "s_sub_u32 s41, s41, 1\n"
      "s_branch L_nr_21071\n"
      "L_nd_21072:\n"
      "v_mfma_f32_16x16x32_bf16 v[2:5], v[112:115], v[224:227], 0\n"
      "v_mfma_f32_16x16x32_bf16 v[6:9], v[116:119], v[224:227], 0\n"
      "v_mfma_f32_16x16x32_bf16 v[10:13], v[120:123], v[224:227], 0\n"
      "v_mfma_f32_16x16x32_bf16 v[14:17], v[124:127], v[224:227], 0\n"
      "v_mfma_f32_16x16x32_bf16 v[2:5], v[128:131], v[228:231], v[2:5]\n"
      "v_mfma_f32_16x16x32_bf16 v[6:9], v[132:135], v[228:231], v[6:9]\n"
      "v_mfma_f32_16x16x32_bf16 v[10:13], v[136:139], v[228:231], v[10:13]\n"
      "v_mfma_f32_16x16x32_bf16 v[14:17], v[140:143], v[228:231], v[14:17]\n"
      "v_mfma_f32_16x16x32_bf16 v[2:5], v[144:147], v[232:235], v[2:5]\n"
      "v_mfma_f32_16x16x32_bf16 v[6:9], v[148:151], v[232:235], v[6:9]\n"
      "v_mfma_f32_16x16x32_bf16 v[10:13], v[152:155], v[232:235], v[10:13]\n"
      "v_mfma_f32_16x16x32_bf16 v[14:17], v[156:159], v[232:235], v[14:17]\n"
      "v_mfma_f32_16x16x32_bf16 v[2:5], v[160:163], v[236:239], v[2:5]\n"
      "v_mfma_f32_16x16x32_bf16 v[6:9], v[164:167], v[236:239], v[6:9]\n"
      "v_mfma_f32_16x16x32_bf16 v[10:13], v[168:171], v[236:239], v[10:13]\n"
      "v_mfma_f32_16x16x32_bf16 v[14:17], v[172:175], v[236:239], v[14:17]\n"
      "v_mfma_f32_16x16x32_bf16 v[2:5], v[176:179], v[240:243], v[2:5]\n"
      "v_mfma_f32_16x16x32_bf16 v[6:9], v[180:183], v[240:243], v[6:9]\n"
      "v_mfma_f32_16x16x32_bf16 v[10:13], v[184:187], v[240:243], v[10:13]\n"
      "v_mfma_f32_16x16x32_bf16 v[14:17], v[188:191], v[240:243], v[14:17]\n"
      "v_mfma_f32_16x16x32_bf16 v[2:5], v[192:195], v[244:247], v[2:5]\n"
      "v_mfma_f32_16x16x32_bf16 v[6:9], v[196:199], v[244:247], v[6:9]\n"
      "v_mfma_f32_16x16x32_bf16 v[10:13], v[200:203], v[244:247], v[10:13]\n"
      "v_mfma_f32_16x16x32_bf16 v[14:17], v[204:207], v[244:247], v[14:17]\n"
      "v_mfma_f32_16x16x32_bf16 v[2:5], v[208:211], v[248:251], v[2:5]\n"
      "v_mfma_f32_16x16x32_bf16 v[6:9], v[212:215], v[248:251], v[6:9]\n"
      "v_mfma_f32_16x16x32_bf16 v[10:13], v[216:219], v[248:251], v[10:13]\n"
      "v_mfma_f32_16x16x32_bf16 v[14:17], v[220:223], v[248:251], v[14:17]\n"
      "global_load_dwordx4 v[224:227], v1, s[70:71] offset:-4096 sc1\n"
      "global_load_dwordx4 v[228:231], v1, s[70:71] offset:-2048 sc1\n"
      "global_load_dwordx4 v[232:235], v1, s[70:71] offset:0 sc1\n"
      "global_load_dwordx4 v[236:239], v1, s[70:71] offset:2048 sc1\n"
      "global_load_dwordx4 v[240:243], v103, s[70:71] offset:-4096 sc1\n"
      "global_load_dwordx4 v[244:247], v103, s[70:71] offset:-2048 sc1\n"
      "global_load_dwordx4 v[248:251], v103, s[70:71] offset:0 sc1\n"
      "s_waitcnt vmcnt(0)\n"
      "s_waitcnt vmcnt(0) lgkmcnt(0)\n"
      "s_nop 15\n"
      "s_branch L_na_20069\n"
      "L_slow_22072:\n"
      "s_waitcnt vmcnt(0)\n"
      "s_sub_u32 s50, s70, 0x1c000\n"
      "s_subb_u32 s51, s71, 0\n"
      "L_nr_23073:\n"
      "global_load_dwordx4 v[224:227], v1, s[50:51] offset:-4096 sc1\n"
      "global_load_dwordx4 v[228:231], v1, s[50:51] offset:-2048 sc1\n"
      "global_load_dwordx4 v[232:235], v1, s[50:51] offset:0 sc1\n"
      "global_load_dwordx4 v[236:239], v1, s[50:51] offset:2048 sc1\n"
      "global_load_dwordx4 v[240:243], v103, s[50:51] offset:-4096 sc1\n"
      "global_load_dwordx4 v[244:247], v103, s[50:51] offset:-2048 sc1\n"
      "global_load_dwordx4 v[248:251], v103, s[50:51] offset:0 sc1\n"
      "v_mov_b32 v111, 0\n"
      "s_waitcnt vmcnt(6)\n"
      "v_max3_u32 v111, v111, v224, v225\n"
      "v_max3_u32 v111, v111, v226, v227\n"
      "s_waitcnt vmcnt(5)\n"
      "v_max3_u32 v111, v111, v228, v229\n"
      "v_max3_u32 v111, v111, v230, v231\n"
      "s_waitcnt vmcnt(4)\n"
      "v_max3_u32 v111, v111, v232, v233\n"
      "v_max3_u32 v111, v111, v234, v235\n"
      "s_waitcnt vmcnt(3)\n"
      "v_max3_u32 v111, v111, v236, v237\n"
      "v_max3_u32 v111, v111, v238, v239\n"
      "s_waitcnt vmcnt(2)\n"
      "v_max3_u32 v111, v111, v240, v241\n"
      "v_max3_u32 v111, v111, v242, v243\n"
      "s_waitcnt vmcnt(1)\n"
      "v_max3_u32 v111, v111, v244, v245\n"
      "v_max3_u32 v111, v111, v246, v247\n"
      "s_waitcnt vmcnt(0)\n"
      "v_max3_u32 v111, v111, v248, v249\n"
      "v_max3_u32 v111, v111, v250, v251\n"
      "v_cmp_eq_u32 vcc, -1, v111\n"
      "s_nop 0\n"
      "s_cbranch_vccz L_nd_23074\n"
      "s_cmp_le_i32 s41, 0\n"
      "s_cbranch_scc1 L_nd_23074\n"
      "s_sub_u32 s41, s41, 1\n"
      "s_branch L_nr_23073\n"
      "L_nd_23074:\n"
      "v_mfma_f32_16x16x32_bf16 v[18:21], v[112:115], v[224:227], 0\n"
      "v_mfma_f32_16x16x32_bf16 v[22:25], v[116:119], v[224:227], 0\n"
      "v_mfma_f32_16x16x32_bf16 v[26:29], v[120:123], v[224:227], 0\n"
      "v_mfma_f32_16x16x32_bf16 v[30:33], v[124:127], v[224:227], 0\n"
      "v_mfma_f32_16x16x32_bf16 v[18:21], v[128:131], v[228:231], v[18:21]\n"
      "v_mfma_f32_16x16x32_bf16 v[22:25], v[132:135], v[228:231], v[22:25]\n"
      "v_mfma_f32_16x16x32_bf16 v[26:29], v[136:139], v[228:231], v[26:29]\n"
      "v_mfma_f32_16x16x32_bf16 v[30:33], v[140:143], v[228:231], v[30:33]\n"
      "v_mfma_f32_16x16x32_bf16 v[18:21], v[144:147], v[232:235], v[18:21]\n"
      "v_mfma_f32_16x16x32_bf16 v[22:25], v[148:151], v[232:235], v[22:25]\n"
      "v_mfma_f32_16x16x32_bf16 v[26:29], v[152:155], v[232:235], v[26:29]\n"
      "v_mfma_f32_16x16x32_bf16 v[30:33], v[156:159], v[232:235], v[30:33]\n"
      "v_mfma_f32_16x16x32_bf16 v[18:21], v[160:163], v[236:239], v[18:21]\n"
      "v_mfma_f32_16x16x32_bf16 v[22:25], v[164:167], v[236:239], v[22:25]\n"
      "v_mfma_f32_16x16x32_bf16 v[26:29], v[168:171], v[236:239], v[26:29]\n"
      "v_mfma_f32_16x16x32_bf16 v[30:33], v[172:175], v[236:239], v[30:33]\n"
      "v_mfma_f32_16x16x32_bf16 v[18:21], v[176:179], v[240:243], v[18:21]\n"
      "v_mfma_f32_16x16x32_bf16 v[22:25], v[180:183], v[240:243], v[22:25]\n"
      "v_mfma_f32_16x16x32_bf16 v[26:29], v[184:187], v[240:243], v[26:29]\n"
      "v_mfma_f32_16x16x32_bf16 v[30:33], v[188:191], v[240:243], v[30:33]\n"
      "v_mfma_f32_16x16x32_bf16 v[18:21], v[192:195], v[244:247], v[18:21]\n"
      "v_mfma_f32_16x16x32_bf16 v[22:25], v[196:199], v[244:247], v[22:25]\n"
      "v_mfma_f32_16x16x32_bf16 v[26:29], v[200:203], v[244:247], v[26:29]\n"
      "v_mfma_f32_16x16x32_bf16 v[30:33], v[204:207], v[244:247], v[30:33]\n"
      "v_mfma_f32_16x16x32_bf16 v[18:21], v[208:211], v[248:251], v[18:21]\n"
      "v_mfma_f32_16x16x32_bf16 v[22:25], v[212:215], v[248:251], v[22:25]\n"
      "v_mfma_f32_16x16x32_bf16 v[26:29], v[216:219], v[248:251], v[26:29]\n"
      "v_mfma_f32_16x16x32_bf16 v[30:33], v[220:223], v[248:251], v[30:33]\n"
      "global_load_dwordx4 v[224:227], v1, s[70:71] offset:-4096 sc1\n"
      "global_load_dwordx4 v[228:231], v1, s[70:71] offset:-2048 sc1\n"
      "global_load_dwordx4 v[232:235], v1, s[70:71] offset:0 sc1\n"
      "global_load_dwordx4 v[236:239], v1, s[70:71] offset:2048 sc1\n"
      "global_load_dwordx4 v[240:243], v103, s[70:71] offset:-4096 sc1\n"
      "global_load_dwordx4 v[244:247], v103, s[70:71] offset:-2048 sc1\n"
      "global_load_dwordx4 v[248:251], v103, s[70:71] offset:0 sc1\n"
      "s_waitcnt vmcnt(0)\n"
      "s_waitcnt vmcnt(0) lgkmcnt(0)\n"
      "s_nop 15\n"
      "s_branch L_na_22071\n"
      "L_slow_24074:\n"
      "s_waitcnt vmcnt(0)\n"
      "s_sub_u32 s50, s70, 0x1c000\n"
      "s_subb_u32 s51, s71, 0\n"
      "L_nr_25075:\n"
      "global_load_dwordx4 v[224:227], v1, s[50:51] offset:-4096 sc1\n"
      "global_load_dwordx4 v[228:231], v1, s[50:51] offset:-2048 sc1\n"
      "global_load_dwordx4 v[232:235], v1, s[50:51] offset:0 sc1\n"
      "global_load_dwordx4 v[236:239], v1, s[50:51] offset:2048 sc1\n"
      "global_load_dwordx4 v[240:243], v103, s[50:51] offset:-4096 sc1\n"
      "global_load_dwordx4 v[244:247], v103, s[50:51] offset:-2048 sc1\n"
      "global_load_dwordx4 v[248:251], v103, s[50:51] offset:0 sc1\n"
      "v_mov_b32 v111, 0\n"
      "s_waitcnt vmcnt(6)\n"
      "v_max3_u32 v111, v111, v224, v225\n"
      "v_max3_u32 v111, v111, v226, v227\n"
      "s_waitcnt vmcnt(5)\n"
      "v_max3_u32 v111, v111, v228, v229\n"
      "v_max3_u32 v111, v111, v230, v231\n"
      "s_waitcnt vmcnt(4)\n"
      "v_max3_u32 v111, v111, v232, v233\n"
      "v_max3_u32 v111, v111, v234, v235\n"
      "s_waitcnt vmcnt(3)\n"
      "v_max3_u32 v111, v111, v236, v237\n"
      "v_max3_u32 v111, v111, v238, v239\n"
      "s_waitcnt vmcnt(2)\n"
      "v_max3_u32 v111, v111, v240, v241\n"
      "v_max3_u32 v111, v111, v242, v243\n"
      "s_waitcnt vmcnt(1)\n"
      "v_max3_u32 v111, v111, v244, v245\n"
      "v_max3_u32 v111, v111, v246, v247\n"
      "s_waitcnt vmcnt(0)\n"
      "v_max3_u32 v111, v111, v248, v249\n"
      "v_max3_u32 v111, v111, v250, v251\n"
      "v_cmp_eq_u32 vcc, -1, v111\n"
      "s_nop 0\n"
      "s_cbranch_vccz L_nd_25076\n"
      "s_cmp_le_i32 s41, 0\n"
      "s_cbranch_scc1 L_nd_25076\n"
      "s_sub_u32 s41, s41, 1\n"
      "s_branch L_nr_25075\n"
      "L_nd_25076:\n"
      "v_mfma_f32_16x16x32_bf16 v[2:5], v[112:115], v[224:227], 0\n"
      "v_mfma_f32_16x16x32_bf16 v[6:9], v[116:119], v[224:227], 0\n"
      "v_mfma_f32_16x16x32_bf16 v[10:13], v[120:123], v[224:227], 0\n"
      "v_mfma_f32_16x16x32_bf16 v[14:17], v[124:127], v[224:227], 0\n"
      "v_mfma_f32_16x16x32_bf16 v[2:5], v[128:131], v[228:231], v[2:5]\n"
      "v_mfma_f32_16x16x32_bf16 v[6:9], v[132:135], v[228:231], v[6:9]\n"
      "v_mfma_f32_16x16x32_bf16 v[10:13], v[136:139], v[228:231], v[10:13]\n"
      "v_mfma_f32_16x16x32_bf16 v[14:17], v[140:143], v[228:231], v[14:17]\n"
      "v_mfma_f32_16x16x32_bf16 v[2:5], v[144:147], v[232:235], v[2:5]\n"
      "v_mfma_f32_16x16x32_bf16 v[6:9], v[148:151], v[232:235], v[6:9]\n"
      "v_mfma_f32_16x16x32_bf16 v[10:13], v[152:155], v[232:235], v[10:13]\n"
      "v_mfma_f32_16x16x32_bf16 v[14:17], v[156:159], v[232:235], v[14:17]\n"
      "v_mfma_f32_16x16x32_bf16 v[2:5], v[160:163], v[236:239], v[2:5]\n"
      "v_mfma_f32_16x16x32_bf16 v[6:9], v[164:167], v[236:239], v[6:9]\n"
      "v_mfma_f32_16x16x32_bf16 v[10:13], v[168:171], v[236:239], v[10:13]\n"
      "v_mfma_f32_16x16x32_bf16 v[14:17], v[172:175], v[236:239], v[14:17]\n"
      "v_mfma_f32_16x16x32_bf16 v[2:5], v[176:179], v[240:243], v[2:5]\n"
      "v_mfma_f32_16x16x32_bf16 v[6:9], v[180:183], v[240:243], v[6:9]\n"
      "v_mfma_f32_16x16x32_bf16 v[10:13], v[184:187], v[240:243], v[10:13]\n"
      "v_mfma_f32_16x16x32_bf16 v[14:17], v[188:191], v[240:243], v[14:17]\n"
      "v_mfma_f32_16x16x32_bf16 v[2:5], v[192:195], v[244:247], v[2:5]\n"
      "v_mfma_f32_16x16x32_bf16 v[6:9], v[196:199], v[244:247], v[6:9]\n"
      "v_mfma_f32_16x16x32_bf16 v[10:13], v[200:203], v[244:247], v[10:13]\n"
      "v_mfma_f32_16x16x32_bf16 v[14:17], v[204:207], v[244:247], v[14:17]\n"
      "v_mfma_f32_16x16x32_bf16 v[2:5], v[208:211], v[248:251], v[2:5]\n"
      "v_mfma_f32_16x16x32_bf16 v[6:9], v[212:215], v[248:251], v[6:9]\n"
      "v_mfma_f32_16x16x32_bf16 v[10:13], v[216:219], v[248:251], v[10:13]\n"
      "v_mfma_f32_16x16x32_bf16 v[14:17], v[220:223], v[248:251], v[14:17]\n"
      "global_load_dwordx4 v[224:227], v1, s[70:71] offset:-4096 sc1\n"
      "global_load_dwordx4 v[228:231], v1, s[70:71] offset:-2048 sc1\n"
      "global_load_dwordx4 v[232:235], v1, s[70:71] offset:0 sc1\n"
      "global_load_dwordx4 v[236:239], v1, s[70:71] offset:2048 sc1\n"
      "global_load_dwordx4 v[240:243], v103, s[70:71] offset:-4096 sc1\n"
      "global_load_dwordx4 v[244:247], v103, s[70:71] offset:-2048 sc1\n"
      "global_load_dwordx4 v[248:251], v103, s[70:71] offset:0 sc1\n"
      "s_waitcnt vmcnt(0)\n"
      "s_waitcnt vmcnt(0) lgkmcnt(0)\n"
      "s_nop 15\n"
      "s_branch L_na_24073\n"
      "L_slow_26076:\n"
      "s_waitcnt vmcnt(0)\n"
      "s_sub_u32 s50, s70, 0\n"
      "s_subb_u32 s51, s71, 0\n"
      "L_nr_27077:\n"
      "global_load_dwordx4 v[224:227], v1, s[50:51] offset:-4096 sc1\n"
      "global_load_dwordx4 v[228:231], v1, s[50:51] offset:-2048 sc1\n"
      "global_load_dwordx4 v[232:235], v1, s[50:51] offset:0 sc1\n"
      "global_load_dwordx4 v[236:239], v1, s[50:51] offset:2048 sc1\n"
      "global_load_dwordx4 v[240:243], v103, s[50:51] offset:-4096 sc1\n"
      "global_load_dwordx4 v[244:247], v103, s[50:51] offset:-2048 sc1\n"
      "global_load_dwordx4 v[248:251], v103, s[50:51] offset:0 sc1\n"
      "v_mov_b32 v111, 0\n"
      "s_waitcnt vmcnt(6)\n"
      "v_max3_u32 v111, v111, v224, v225\n"
      "v_max3_u32 v111, v111, v226, v227\n"
      "s_waitcnt vmcnt(5)\n"
      "v_max3_u32 v111, v111, v228, v229\n"
      "v_max3_u32 v111, v111, v230, v231\n"
      "s_waitcnt vmcnt(4)\n"
      "v_max3_u32 v111, v111, v232, v233\n"
      "v_max3_u32 v111, v111, v234, v235\n"
      "s_waitcnt vmcnt(3)\n"
      "v_max3_u32 v111, v111, v236, v237\n"
      "v_max3_u32 v111, v111, v238, v239\n"
      "s_waitcnt vmcnt(2)\n"
      "v_max3_u32 v111, v111, v240, v241\n"
      "v_max3_u32 v111, v111, v242, v243\n"
      "s_waitcnt vmcnt(1)\n"
      "v_max3_u32 v111, v111, v244, v245\n"
      "v_max3_u32 v111, v111, v246, v247\n"
      "s_waitcnt vmcnt(0)\n"
      "v_max3_u32 v111, v111, v248, v249\n"
      "v_max3_u32 v111, v111, v250, v251\n"
      "v_cmp_eq_u32 vcc, -1, v111\n"
      "s_nop 0\n"
      "s_cbranch_vccz L_nd_27078\n"
      "s_cmp_le_i32 s41, 0\n"
      "s_cbranch_scc1 L_nd_27078\n"
      "s_sub_u32 s41, s41, 1\n"
      "s_branch L_nr_27077\n"
      "L_nd_27078:\n"
      "v_mfma_f32_16x16x32_bf16 v[18:21], v[112:115], v[224:227], 0\n"
      "v_mfma_f32_16x16x32_bf16 v[22:25], v[116:119], v[224:227], 0\n"
      "v_mfma_f32_16x16x32_bf16 v[26:29], v[120:123], v[224:227], 0\n"
      "v_mfma_f32_16x16x32_bf16 v[30:33], v[124:127], v[224:227], 0\n"
      "v_mfma_f32_16x16x32_bf16 v[18:21], v[128:131], v[228:231], v[18:21]\n"
      "v_mfma_f32_16x16x32_bf16 v[22:25], v[132:135], v[228:231], v[22:25]\n"
      "v_mfma_f32_16x16x32_bf16 v[26:29], v[136:139], v[228:231], v[26:29]\n"
      "v_mfma_f32_16x16x32_bf16 v[30:33], v[140:143], v[228:231], v[30:33]\n"
      "v_mfma_f32_16x16x32_bf16 v[18:21], v[144:147], v[232:235], v[18:21]\n"
      "v_mfma_f32_16x16x32_bf16 v[22:25], v[148:151], v[232:235], v[22:25]\n"
      "v_mfma_f32_16x16x32_bf16 v[26:29], v[152:155], v[232:235], v[26:29]\n"
      "v_mfma_f32_16x16x32_bf16 v[30:33], v[156:159], v[232:235], v[30:33]\n"
      "v_mfma_f32_16x16x32_bf16 v[18:21], v[160:163], v[236:239], v[18:21]\n"
      "v_mfma_f32_16x16x32_bf16 v[22:25], v[164:167], v[236:239], v[22:25]\n"
      "v_mfma_f32_16x16x32_bf16 v[26:29], v[168:171], v[236:239], v[26:29]\n"
      "v_mfma_f32_16x16x32_bf16 v[30:33], v[172:175], v[236:239], v[30:33]\n"
      "v_mfma_f32_16x16x32_bf16 v[18:21], v[176:179], v[240:243], v[18:21]\n"
      "v_mfma_f32_16x16x32_bf16 v[22:25], v[180:183], v[240:243], v[22:25]\n"
      "v_mfma_f32_16x16x32_bf16 v[26:29], v[184:187], v[240:243], v[26:29]\n"
      "v_mfma_f32_16x16x32_bf16 v[30:33], v[188:191], v[240:243], v[30:33]\n"
      "v_mfma_f32_16x16x32_bf16 v[18:21], v[192:195], v[244:247], v[18:21]\n"
      "v_mfma_f32_16x16x32_bf16 v[22:25], v[196:199], v[244:247], v[22:25]\n"
      "v_mfma_f32_16x16x32_bf16 v[26:29], v[200:203], v[244:247], v[26:29]\n"
      "v_mfma_f32_16x16x32_bf16 v[30:33], v[204:207], v[244:247], v[30:33]\n"
      "v_mfma_f32_16x16x32_bf16 v[18:21], v[208:211], v[248:251], v[18:21]\n"
      "v_mfma_f32_16x16x32_bf16 v[22:25], v[212:215], v[248:251], v[22:25]\n"
      "v_mfma_f32_16x16x32_bf16 v[26:29], v[216:219], v[248:251], v[26:29]\n"
      "v_mfma_f32_16x16x32_bf16 v[30:33], v[220:223], v[248:251], v[30:33]\n"
      "s_waitcnt vmcnt(0) lgkmcnt(0)\n"
      "s_nop 15\n"
      "s_branch L_na_26075\n"
      "L_codeend:\n"
      :
      : "{s[0:1]}"(__builtin_amdgcn_kernarg_segment_ptr()), "{s2}"(blockIdx.x), "{v0}"(threadIdx.x), "{s[20:21]}"(kTab)
      : "v1", "v2", "v3", "v4", "v5", "v6", "v7", "v8", "v9", "v10", "v11", "v12", "v13", "v14", "v15", "v16", "v17", "v18", "v19", "v20", "v21", "v22", "v23", "v24", "v25", "v26", "v27", "v28", "v29", "v30", "v31", "v32", "v33", "v34", "v35", "v36", "v37", "v38", "v39", "v40", "v41", "v42", "v43", "v44", "v45", "v46", "v47", "v48", "v49", "v50", "v51", "v52", "v53", "v54", "v55", "v56", "v57", "v58", "v59", "v60", "v61", "v62", "v63", "v64", "v65", "v66", "v67", "v68", "v69", "v70", "v71", "v72", "v73", "v74", "v75", "v76", "v77", "v78", "v79", "v80", "v81", "v82", "v83", "v84", "v85", "v86", "v87", "v88", "v89", "v90", "v91", "v92", "v93", "v94", "v95", "v96", "v97", "v98", "v99", "v100", "v101", "v102", "v103", "v104", "v105", "v106", "v107", "v108", "v109", "v110", "v111", "v112", "v113", "v114", "v115", "v116", "v117", "v118", "v119", "v120", "v121", "v122", "v123", "v124", "v125", "v126", "v127", "v128", "v129", "v130", "v131", "v132", "v133", "v134", "v135", "v136", "v137", "v138", "v139", "v140", "v141", "v142", "v143", "v144", "v145", "v146", "v147", "v148", "v149", "v150", "v151", "v152", "v153", "v154", "v155", "v156", "v157", "v158", "v159", "v160", "v161", "v162", "v163", "v164", "v165", "v166", "v167", "v168", "v169", "v170", "v171", "v172", "v173", "v174", "v175", "v176", "v177", "v178", "v179", "v180", "v181", "v182", "v183", "v184", "v185", "v186", "v187", "v188", "v189", "v190", "v191", "v192", "v193", "v194", "v195", "v196", "v197", "v198", "v199", "v200", "v201", "v202", "v203", "v204", "v205", "v206", "v207", "v208", "v209", "v210", "v211", "v212", "v213", "v214", "v215", "v216", "v217", "v218", "v219", "v220", "v221", "v222", "v223", "v224", "v225", "v226", "v227", "v228", "v229", "v230", "v231", "v232", "v233", "v234", "v235", "v236", "v237", "v238", "v239", "v240", "v241", "v242", "v243", "v244", "v245", "v246", "v247", "v248", "v249", "v250", "v251", "v252", "v253", "v254", "v255", "s3", "s4", "s5", "s6", "s7", "s8", "s9", "s10", "s11", "s12", "s13", "s14", "s15", "s16", "s17", "s18", "s19", "s22", "s23", "s24", "s25", "s26", "s27", "s28", "s29", "s30", "s31", "s33", "s34", "s35", "s36", "s37", "s38", "s39", "s40", "s41", "s42", "s43", "s44", "s45", "s46", "s47", "s48", "s49", "s50", "s51", "s52", "s53", "s54", "s55", "s56", "s57", "s58", "s59", "s60", "s61", "s62", "s63", "s64", "s65", "s66", "s67", "s68", "s69", "s70", "s71", "s72", "s73", "s74", "s75", "s76", "s77", "s78", "s79", "s80", "s81", "s82", "s83", "s84", "s85", "s86", "s87", "s88", "s89", "s90", "s91", "s92", "s93", "s94", "s95", "s96", "s97", "s98", "s99", "vcc", "scc", "memory");
#endif
}

extern "C" void kernel_launch(void* const* d_in, const int* in_sizes, int n_in, void* d_out, int out_size, void* d_ws, size_t ws_size,
                              hipStream_t stream) {
  Params p;
  p.x = (const u16*)d_in[0]; p.are = (const u16*)d_in[1]; p.aim = (const u16*)d_in[2]; p.bre = (const u16*)d_in[3];
  p.bim = (const u16*)d_in[4]; p.cre = (const u16*)d_in[5]; p.cim = (const u16*)d_in[6]; p.ldt = (const u16*)d_in[7];
  p.dsk = (const u16*)d_in[8]; p.w1 = (const u16*)d_in[10]; p.b1 = (const u16*)d_in[11]; p.w2 = (const u16*)d_in[12];
  p.out = (u16*)d_out; p.ws = (unsigned char*)d_ws + WS_SHIFT(ws_size);
  (void)hipFuncSetAttribute((const void*)s4_mlp_kernel, hipFuncAttributeMaxDynamicSharedMemorySize, LDS_BYTES);
  s4_mlp_kernel<<<256, 512, LDS_BYTES, stream>>>(p);
}
```
